```python
import torch

def _softmax(x, axis=-1):
    return torch.softmax(x, dim=axis)
def _gelu(x):
    return torch.nn.functional.gelu(x, approximate="tanh")
def _silu(x):
    return torch.nn.functional.silu(x)
def _sigmoid(x):
    return torch.sigmoid(x)
def _layernorm(x, axis=-1, eps=1e-5):
    m = x.mean(dim=axis, keepdim=True)
    v = x.var(dim=axis, keepdim=True, unbiased=False)
    return (x - m) / torch.sqrt(v + eps)
def _scatter_add(tgt, idx, src, axis=0):
    return tgt.clone().index_add_(axis, idx, src)
def _segment_sum(src, seg, n):
    out = torch.zeros((n,) + src.shape[1:], dtype=src.dtype)
    return out.index_add_(0, seg, src)
def _pad(x, pw):
    flat = tuple(v for pair in reversed(pw) for v in pair)
    return torch.nn.functional.pad(x, flat)

kernel_name = 'interleave_layers__spectral_gfnet__dense_activation_gelu__prompt'

def setup_inputs():

    _w = (lambda a: (lambda t: t.to(torch.bfloat16) if t.is_floating_point() else t)(torch.as_tensor(a)))
    def _A_setup():
        B, S, D = 1, 2048, 640
        Df = D // 2 + 1
        return {
            "x": _w((lambda *s: torch.randn(*s, dtype=torch.bfloat16) * 0.05)(B, S, D)),
            "w_r": _w((lambda *s: torch.randn(*s, dtype=torch.bfloat16) * 0.4)(S, Df) * 0.02),
            "w_i": _w((lambda *s: torch.randn(*s, dtype=torch.bfloat16) * 0.4)(S, Df) * 0.02),
        }
    _A_in = _A_setup()

    def _B_setup():
        B, S, D, F = 1, 2048, 640, 1536
        return {
            "x": _w((lambda *s: torch.randn(*s, dtype=torch.bfloat16) * 0.05)(B, S, D)),
            "w1": _w((lambda *s: torch.randn(*s, dtype=torch.bfloat16) * 0.4)(D, F) * (D ** -0.5)),
            "b1": _w((lambda *s: torch.randn(*s, dtype=torch.bfloat16) * 12.8)(F) * 0.01),
            "w2": _w((lambda *s: torch.randn(*s, dtype=torch.bfloat16) * 0.1)(F, D) * (F ** -0.5)),
            "b2": _w((lambda *s: torch.randn(*s, dtype=torch.bfloat16) * 0.8)(D) * 0.01),
        }
    _B_in = _B_setup()

    return {
        "A_x": _A_in["x"],
        "A_w_r": _A_in["w_r"],
        "A_w_i": _A_in["w_i"],
        "B_x": _B_in["x"],
        "B_w1": _B_in["w1"],
        "B_b1": _B_in["b1"],
        "B_w2": _B_in["w2"],
        "B_b2": _B_in["b2"],
    }

def reference(A_x, A_w_r, A_w_i, B_x, B_w1, B_b1, B_w2, B_b2):
    def _A_ref(x, w_r, w_i):
        B, S, D = x.shape
        X = (lambda x, axes=(-2,-1): torch.fft.rfft2(x.float(), dim=axes))(x, axes=(-2, -1))
        W = w_r + 1j * w_i
        X = X * W
        y = (lambda x, s, axes=(-2,-1): torch.fft.irfft2(x, s=s, dim=axes).to(torch.bfloat16))(X, (S, D), axes=(-2, -1))
        return y

    def _B_ref(x, w1, b1, w2, b2):
        h = torch.einsum("bsd,df->bsf", x, w1) + b1
        h = _gelu(h)
        return torch.einsum("bsf,fd->bsd", h, w2) + b2

    x = A_x
    x = x + _A_ref(_layernorm(x, axis=-1), A_w_r, A_w_i)
    x = x + _B_ref(_layernorm(x, axis=-1), B_w1, B_b1, B_w2, B_b2)
    x = x + _A_ref(_layernorm(x, axis=-1), A_w_r, A_w_i)
    x = x + _B_ref(_layernorm(x, axis=-1), B_w1, B_b1, B_w2, B_b2)
    return x
```

```cpp
#include <hip/hip_runtime.h>
#include <stdint.h>
#include <type_traits>

#define GAS __attribute__((address_space(1)))
#define LAS __attribute__((address_space(3)))
typedef unsigned v4u __attribute__((ext_vector_type(4)));
typedef unsigned v2u __attribute__((ext_vector_type(2)));
typedef float f32x4 __attribute__((ext_vector_type(4)));
typedef float f32x2 __attribute__((ext_vector_type(2)));
typedef short s16x4 __attribute__((ext_vector_type(4)));
typedef __bf16 bf16x2 __attribute__((ext_vector_type(2)));
typedef __bf16 bf16x8 __attribute__((ext_vector_type(8)));
typedef GAS unsigned gu32;
#define RLX_AGENT __ATOMIC_RELAXED, __HIP_MEMORY_SCOPE_AGENT

constexpr int S_ = 2048, D_ = 640, F_ = 1536;
constexpr int AUX_SC1 = 16;
constexpr int RSRC_W3 = 0x00020000;

constexpr unsigned OFF_CTRL = 0;
constexpr unsigned OFF_SS = 65536;
constexpr unsigned OFF_B2P = OFF_SS + 32768;
constexpr unsigned XH_BYTES = D_ * S_ * 2;
constexpr unsigned OFF_XH = 131072;
constexpr unsigned H_BYTES = S_ * F_ * 2;
constexpr unsigned OFF_H = OFF_XH + 4 * XH_BYTES;
constexpr unsigned WF_BYTES = D_ * F_ * 2;
constexpr unsigned OFF_W1F = OFF_H + 2 * H_BYTES;
constexpr unsigned OFF_W2F = OFF_W1F + WF_BYTES;
constexpr unsigned OFF_W2T = OFF_W2F + WF_BYTES;
constexpr unsigned OFF_RB2 = OFF_W2T + WF_BYTES;
constexpr unsigned OFF_WT = OFF_RB2 + D_ * D_ * 2;
constexpr unsigned WS_USED = OFF_WT + 320 * 8192;

constexpr int CW_TMO = 0, CW_BAR = 1024, CW_GRP = 8192;
constexpr unsigned SPIN_CAP = 1u << 16;

constexpr int L_TILE = 0;
constexpr int L_PART = 21760;
constexpr int L_MISC = 4 * L_PART;
constexpr int L_SS = L_MISC;
constexpr int L_SSP = L_MISC + 256;
constexpr int L_FLAG = L_MISC + 256 + 1024;
constexpr int L_BYTES = L_MISC + 2048;

template <int I, int N, class Fn> __device__ __forceinline__ void static_for(Fn &&f) {
    if constexpr (I < N) {
        f(std::integral_constant<int, I>{});
        static_for<I + 1, N>(f);
    }
}

__device__ __forceinline__ float bf_lo(unsigned u) { return __builtin_bit_cast(float, u << 16); }
__device__ __forceinline__ float bf_hi(unsigned u) { return __builtin_bit_cast(float, u & 0xFFFF0000u); }
__device__ __forceinline__ unsigned pk_bf(float lo, float hi) {
    f32x2 v = {lo, hi};
    return __builtin_bit_cast(unsigned, __builtin_convertvector(v, bf16x2));
}
__host__ __device__ __forceinline__ int pos2tok(int p) {
    const int l = p >> 3, t = (p >> 2) & 1, r = p & 3;
    const int w = l >> 6, g = (l >> 4) & 3, bl = l & 15;
    return 32 * (16 * w + 4 * g + r) + 16 * t + bl;
}

#define MFMA_OP "v_mfma_f32_16x16x32_bf16 "
template <int Q> __device__ __forceinline__ void mf_ra0(f32x4 &c, v4u b) {
    asm volatile(MFMA_OP "%0, a[%c2:%c3], %1, 0" : "=&v"(c) : "v"(b), "i"(4 * Q), "i"(4 * Q + 3));
}
template <int Q> __device__ __forceinline__ void mf_ra(f32x4 &c, v4u b) {
    asm volatile(MFMA_OP "%0, a[%c2:%c3], %1, %0" : "+v"(c) : "v"(b), "i"(4 * Q), "i"(4 * Q + 3));
}
template <int Q> __device__ __forceinline__ void mf_rb0(f32x4 &c, v4u a) {
    asm volatile(MFMA_OP "%0, %1, a[%c2:%c3], 0" : "=&v"(c) : "v"(a), "i"(4 * Q), "i"(4 * Q + 3));
}
template <int Q> __device__ __forceinline__ void mf_rb(f32x4 &c, v4u a) {
    asm volatile(MFMA_OP "%0, %1, a[%c2:%c3], %0" : "+v"(c) : "v"(a), "i"(4 * Q), "i"(4 * Q + 3));
}
__device__ __forceinline__ void mf_vv0(f32x4 &c, v4u a, v4u b) {
    asm volatile(MFMA_OP "%0, %1, %2, 0" : "=&v"(c) : "v"(a), "v"(b));
}
__device__ __forceinline__ void mf_vv(f32x4 &c, v4u a, v4u b) {
    asm volatile(MFMA_OP "%0, %1, %2, %0" : "+v"(c) : "v"(a), "v"(b));
}
__device__ __forceinline__ void mf_cplx2(f32x4 &c, v4u a0, v4u b0, v4u a1, v4u b1) {
    asm volatile("s_nop 1\n\t" MFMA_OP "%0, %1, %2, 0\n\t" MFMA_OP "%0, %3, %4, %0"
                 : "=&v"(c) : "v"(a0), "v"(b0), "v"(a1), "v"(b1));
}
__device__ __forceinline__ void mf_cplx4(f32x4 &c, v4u a0, v4u b0, v4u a1, v4u b1, v4u a2, v4u b2, v4u a3, v4u b3) {
    asm volatile("s_nop 1\n\t" MFMA_OP "%0, %1, %2, 0\n\t" MFMA_OP "%0, %3, %4, %0\n\t" MFMA_OP "%0, %5, %6, %0\n\t" MFMA_OP "%0, %7, %8, %0"
                 : "=&v"(c) : "v"(a0), "v"(b0), "v"(a1), "v"(b1), "v"(a2), "v"(b2), "v"(a3), "v"(b3));
}
#define MF_FENCE4(a, b, c, d) asm volatile("s_nop 7" : "+v"(a), "+v"(b), "+v"(c), "+v"(d))

template <int Q, int IMM> __device__ __forceinline__ void ring_ld(const GAS unsigned char *sbase, unsigned voff) {
    asm volatile("global_load_dwordx4 a[%c2:%c3], %0, %1 offset:%c4" ::"v"(voff), "s"(sbase), "i"(4 * Q), "i"(4 * Q + 3), "i"(IMM));
}
template <int Q> __device__ __forceinline__ void ring_ld_q(const GAS unsigned char *run, unsigned voff) {
    ring_ld<Q, (Q & 3) * 1024>(run + (Q >> 2) * 4096, voff);
}
template <int LO, int HI> __device__ __forceinline__ void ring_fill(const GAS unsigned char *run, unsigned voff) {
    static_for<LO, HI>([&](auto q) { ring_ld_q<q.value>(run, voff); });
}
#define DRAIN() asm volatile("s_waitcnt vmcnt(0)" ::: "memory")

__device__ __forceinline__ v4u ld_sc1(__amdgpu_buffer_rsrc_t rs, int voff, int soff) {
    return __builtin_amdgcn_raw_buffer_load_b128(rs, voff, soff, AUX_SC1);
}
__device__ __forceinline__ v4u ld_pl(__amdgpu_buffer_rsrc_t rs, int voff, int soff) {
    return __builtin_amdgcn_raw_buffer_load_b128(rs, voff, soff, 0);
}
__device__ __forceinline__ void st_sc1(v4u v, __amdgpu_buffer_rsrc_t rs, int voff, int soff) {
    __builtin_amdgcn_raw_buffer_store_b128(v, rs, voff, soff, AUX_SC1);
}
__device__ __forceinline__ v4u lds_rd128(LAS unsigned char *L, int off) { return *(LAS v4u *)(L + off); }
__device__ __forceinline__ v2u lds_tr(LAS unsigned char *L, int off) {
    return __builtin_bit_cast(v2u, __builtin_amdgcn_ds_read_tr16_b64_v4i16((LAS s16x4 *)(L + off)));
}

struct Params {
    const uint16_t *x, *wr, *wi, *w1, *b1, *w2, *b2;
    uint16_t *out;
    unsigned char *ws;
};

__device__ __forceinline__ void signal_add(gu32 *word) {
    DRAIN();
    __syncthreads();
    if (threadIdx.x == 0) __hip_atomic_fetch_add(word, 1u, RLX_AGENT);
}
template <int NSH> __device__ __forceinline__ bool wait_words(gu32 *words, unsigned want, gu32 *tmo, LAS unsigned char *L) {
    volatile LAS unsigned *flag = (volatile LAS unsigned *)(L + L_FLAG);
    if (threadIdx.x < 64) {
        const int lane = threadIdx.x;
        unsigned ok = 1u;
        for (unsigned spins = 0;;) {
            unsigned v = want;
            if (lane < NSH) v = __hip_atomic_load(words + lane * 64, RLX_AGENT);
            if (__all(v >= want)) break;
            if (++spins > SPIN_CAP) {
                if (lane == 0) __hip_atomic_store(tmo, 1u, RLX_AGENT);
                ok = 0u;
                break;
            }
            if ((spins & 255u) == 0u && __hip_atomic_load(tmo, RLX_AGENT) != 0u) { ok = 0u; break; }
        }
        __builtin_amdgcn_fence(__ATOMIC_ACQUIRE, "wavefront");
        if (lane == 0) *flag = ok;
    }
    __syncthreads();
    return *flag != 0u;
}

struct CConst {
    v4u Cr[2], Ci[2], nCi[2];
    v4u Er[2], Ei[2], nEi[2];
    float t1r[8], t1i[8], t2r[8], t2i[8];
};
__device__ __forceinline__ void c_consts(CConst &K, int w, int lane) {
    const int g = lane >> 4, i = lane & 15;
#pragma unroll
    for (int ks = 0; ks < 2; ++ks) {
        float c[8], s[8];
#pragma unroll
        for (int j = 0; j < 8; ++j) {
            const float r = (float)(((32 * ks + 8 * g + j) * (16 * w + i)) & 63) * (1.0f / 64.0f);
            c[j] = __builtin_amdgcn_cosf(r);
            s[j] = __builtin_amdgcn_sinf(r);
        }
#pragma unroll
        for (int h = 0; h < 4; ++h) {
            K.Cr[ks][h] = pk_bf(c[2 * h], c[2 * h + 1]);
            K.nCi[ks][h] = pk_bf(s[2 * h], s[2 * h + 1]);
            K.Ci[ks][h] = K.nCi[ks][h] ^ 0x80008000u;
        }
    }
#pragma unroll
    for (int td = 0; td < 2; ++td) {
        float c[8], s[8];
#pragma unroll
        for (int j = 0; j < 8; ++j) {
            const float r = (float)(((16 * (j >> 2) + 4 * g + (j & 3)) * (16 * td + i)) & 31) * (1.0f / 32.0f);
            c[j] = __builtin_amdgcn_cosf(r);
            s[j] = __builtin_amdgcn_sinf(r);
        }
#pragma unroll
        for (int h = 0; h < 4; ++h) {
            K.Er[td][h] = pk_bf(c[2 * h], c[2 * h + 1]);
            K.nEi[td][h] = pk_bf(s[2 * h], s[2 * h + 1]);
            K.Ei[td][h] = K.nEi[td][h] ^ 0x80008000u;
        }
    }
#pragma unroll
    for (int e = 0; e < 8; ++e) {
        const int tb = e >> 2, r = e & 3;
        const float a1 = (float)(((16 * tb + 4 * g + r) * (16 * w + i)) & 2047) * (1.0f / 2048.0f);
        K.t1r[e] = __builtin_amdgcn_cosf(a1);
        K.t1i[e] = -__builtin_amdgcn_sinf(a1);
        const float a2 = (float)(((16 * tb + i) * (16 * w + 4 * g + r)) & 2047) * (1.0f / 2048.0f);
        K.t2r[e] = __builtin_amdgcn_cosf(a2);
        K.t2i[e] = __builtin_amdgcn_sinf(a2);
    }
}

__device__ __forceinline__ void cmul_pack(const f32x4 (&re)[2], const f32x4 (&im)[2], const float (&tr)[8], const float (&ti)[8], v4u &ore, v4u &oim) {
    float a[8], b[8];
#pragma unroll
    for (int e = 0; e < 8; ++e) {
        const float x = re[e >> 2][e & 3], y = im[e >> 2][e & 3];
        a[e] = x * tr[e] - y * ti[e];
        b[e] = x * ti[e] + y * tr[e];
    }
#pragma unroll
    for (int h = 0; h < 4; ++h) {
        ore[h] = pk_bf(a[2 * h], a[2 * h + 1]);
        oim[h] = pk_bf(b[2 * h], b[2 * h + 1]);
    }
}

template <bool SC1>
__device__ __forceinline__ void phase_c(const CConst &K, LAS unsigned char *L, __amdgpu_buffer_rsrc_t rs, int q, int layer, int w, int lane) {
    const int g = lane >> 4, bl = lane & 15;
    const unsigned xin = OFF_XH + (2 * layer) * XH_BYTES, xout = OFF_XH + (2 * layer + 1) * XH_BYTES;
    const int voff = (64 * w + lane) * 16;
    v4u xr[2], xi[2], wr[2], wi[2], ssv[4];
#pragma unroll
    for (int c = 0; c < 2; ++c) {
        xr[c] = __builtin_amdgcn_raw_buffer_load_b128(rs, voff, xin + (4 * q + 2 * c) * 4096, SC1 ? AUX_SC1 : 0);
        xi[c] = __builtin_amdgcn_raw_buffer_load_b128(rs, voff, xin + (4 * q + 2 * c + 1) * 4096, SC1 ? AUX_SC1 : 0);
    }
#pragma unroll
    for (int h = 0; h < 4; ++h)
        ssv[h] = __builtin_amdgcn_raw_buffer_load_b128(rs, (64 * w + lane) * 64 + h * 16, OFF_SS + layer * 16384, SC1 ? AUX_SC1 : 0);
#pragma unroll
    for (int c = 0; c < 2; ++c) {
        wr[c] = ld_pl(rs, voff, OFF_WT + (2 * q + c) * 8192);
        wi[c] = ld_pl(rs, voff, OFF_WT + (2 * q + c) * 8192 + 4096);
    }
    float rstd[8];
#pragma unroll
    for (int e = 0; e < 8; ++e) {
        const v2u u = {ssv[e >> 1][(e & 1) * 2], ssv[e >> 1][(e & 1) * 2 + 1]};
        const float ss = (float)__builtin_bit_cast(double, u);
        rstd[e] = __builtin_amdgcn_rsqf(ss * (1.0f / 640.0f) + 1e-5f);
    }
    const int wadr = bl * 128 + (((2 * w + (g >> 1)) ^ ((bl >> 1) & 7)) * 16) + (g & 1) * 8;
    const int radr0 = bl * 128 + ((g ^ ((bl >> 1) & 7)) * 16), radr1 = bl * 128 + (((4 + g) ^ ((bl >> 1) & 7)) * 16);
    const bool col0 = (q == 0);
#pragma unroll
    for (int c = 0; c < 2; ++c) {
        float zr[8], zi[8];
#pragma unroll
        for (int h = 0; h < 4; ++h) {
            zr[2 * h] = bf_lo(xr[c][h]) * rstd[2 * h];
            zr[2 * h + 1] = bf_hi(xr[c][h]) * rstd[2 * h + 1];
            zi[2 * h] = bf_lo(xi[c][h]) * rstd[2 * h];
            zi[2 * h + 1] = bf_hi(xi[c][h]) * rstd[2 * h + 1];
        }
        if (c == 0 && col0) {
#pragma unroll
            for (int e = 0; e < 8; ++e) zi[e] = 0.f;
        }
#pragma unroll
        for (int t = 0; t < 2; ++t) {
            const v2u a = {pk_bf(zr[4 * t], zr[4 * t + 1]), pk_bf(zr[4 * t + 2], zr[4 * t + 3])};
            const v2u b = {pk_bf(zi[4 * t], zi[4 * t + 1]), pk_bf(zi[4 * t + 2], zi[4 * t + 3])};
            *(LAS v2u *)(L + c * 8192 + t * 2048 + wadr) = a;
            *(LAS v2u *)(L + c * 8192 + 4096 + t * 2048 + wadr) = b;
        }
    }
    __syncthreads();
#pragma unroll
    for (int c = 0; c < 2; ++c) {
        f32x4 ur[2], ui[2];
#pragma unroll
        for (int tb = 0; tb < 2; ++tb) {
            const v4u r0 = lds_rd128(L, c * 8192 + tb * 2048 + radr0), r1 = lds_rd128(L, c * 8192 + tb * 2048 + radr1);
            const v4u i0 = lds_rd128(L, c * 8192 + 4096 + tb * 2048 + radr0), i1 = lds_rd128(L, c * 8192 + 4096 + tb * 2048 + radr1);
            mf_cplx4(ur[tb], r0, K.Cr[0], r1, K.Cr[1], i0, K.nCi[0], i1, K.nCi[1]);
            mf_cplx4(ui[tb], r0, K.Ci[0], r1, K.Ci[1], i0, K.Cr[0], i1, K.Cr[1]);
        }
        MF_FENCE4(ur[0], ur[1], ui[0], ui[1]);
        v4u vr, vi;
        cmul_pack(ur, ui, K.t1r, K.t1i, vr, vi);
        f32x4 Xr[2], Xi[2];
#pragma unroll
        for (int td = 0; td < 2; ++td) {
            mf_cplx2(Xr[td], K.Er[td], vr, K.nEi[td], vi);
            mf_cplx2(Xi[td], K.Ei[td], vr, K.Er[td], vi);
        }
        MF_FENCE4(Xr[0], Xr[1], Xi[0], Xi[1]);
        float fr[8], fi[8];
#pragma unroll
        for (int h = 0; h < 4; ++h) {
            fr[2 * h] = bf_lo(wr[c][h]); fr[2 * h + 1] = bf_hi(wr[c][h]);
            fi[2 * h] = bf_lo(wi[c][h]); fi[2 * h + 1] = bf_hi(wi[c][h]);
        }
        v4u yr, yi;
        cmul_pack(Xr, Xi, fr, fi, yr, yi);
        f32x4 pr[2], pi[2];
#pragma unroll
        for (int tb = 0; tb < 2; ++tb) {
            mf_cplx2(pr[tb], yr, K.Er[tb], yi, K.Ei[tb]);
            mf_cplx2(pi[tb], yr, K.nEi[tb], yi, K.Er[tb]);
        }
        MF_FENCE4(pr[0], pr[1], pi[0], pi[1]);
        v4u pre, pim;
        cmul_pack(pr, pi, K.t2r, K.t2i, pre, pim);
#pragma unroll
        for (int t = 0; t < 2; ++t) {
            *(LAS v2u *)(L + 16384 + c * 8192 + t * 2048 + wadr) = v2u{pre[2 * t], pre[2 * t + 1]};
            *(LAS v2u *)(L + 16384 + c * 8192 + 4096 + t * 2048 + wadr) = v2u{pim[2 * t], pim[2 * t + 1]};
        }
    }
    __syncthreads();
#pragma unroll
    for (int c = 0; c < 2; ++c) {
        f32x4 or_[2], oi[2];
#pragma unroll
        for (int tb = 0; tb < 2; ++tb) {
            const int base = 16384 + c * 8192 + tb * 2048;
            const v4u r0 = lds_rd128(L, base + radr0), r1 = lds_rd128(L, base + radr1);
            const v4u i0 = lds_rd128(L, base + 4096 + radr0), i1 = lds_rd128(L, base + 4096 + radr1);
            mf_cplx4(or_[tb], K.Cr[0], r0, K.Cr[1], r1, K.Ci[0], i0, K.Ci[1], i1);
            mf_cplx4(oi[tb], K.Cr[0], i0, K.Cr[1], i1, K.nCi[0], r0, K.nCi[1], r1);
        }
        MF_FENCE4(or_[0], or_[1], oi[0], oi[1]);
        v4u orr, oii;
#pragma unroll
        for (int h = 0; h < 4; ++h) {
            const int t = h >> 1, r = (h & 1) * 2;
            orr[h] = pk_bf(bf_lo(xr[c][h]) + or_[t][r], bf_hi(xr[c][h]) + or_[t][r + 1]);
            oii[h] = pk_bf(bf_lo(xi[c][h]) + oi[t][r], bf_hi(xi[c][h]) + oi[t][r + 1]);
        }
        if (c == 0 && col0) oii = xi[0];
        st_sc1(orr, rs, voff, xout + (4 * q + 2 * c) * 4096);
        st_sc1(oii, rs, voff, xout + (4 * q + 2 * c + 1) * 4096);
    }
}

__device__ __forceinline__ float gelu_f(float x) {
    const float t = x * (x * x * -0.10294324f + -2.3022082f);
    return x * __builtin_amdgcn_rcpf(1.0f + __builtin_amdgcn_exp2f(t));
}

template <bool LAST>
__device__ __forceinline__ void phase_g1(const Params &p, LAS unsigned char *L, __amdgpu_buffer_rsrc_t rs, int i, int j, int w, int lane,
                                         const GAS unsigned char *nxt, v4u (&resid)[3]) {
    const int g = lane >> 4, n = lane & 15;
    const unsigned xin = OFF_XH + (LAST ? 3 : 1) * XH_BYTES;
    {
        const int row = 8 * w + (lane >> 3), ch = lane & 7;
        const int voff = row * 4096 + i * 128 + ch * 16;
        const int ladr = row * 128 + (((ch >> 1) ^ ((row >> 1) & 3)) * 32) + (ch & 1) * 16;
        v4u t[20];
#pragma unroll
        for (int ks = 0; ks < 20; ++ks) t[ks] = ld_sc1(rs, voff, xin + ks * 32 * 4096);
#pragma unroll
        for (int ks = 0; ks < 20; ++ks) *(LAS v4u *)(L + ks * 4096 + ladr) = t[ks];
    }
    float b1v[12];
#pragma unroll
    for (int r = 0; r < 3; ++r) {
        const v2u u = *(const GAS v2u *)((const GAS unsigned char *)p.b1 + (192 * j + 48 * w + 16 * r + 4 * g) * 2);
        b1v[4 * r] = bf_lo(u.x); b1v[4 * r + 1] = bf_hi(u.x); b1v[4 * r + 2] = bf_lo(u.y); b1v[4 * r + 3] = bf_hi(u.y);
    }
    asm volatile("" : "+v"(b1v[0]), "+v"(b1v[1]), "+v"(b1v[2]), "+v"(b1v[3]), "+v"(b1v[4]), "+v"(b1v[5]), "+v"(b1v[6]), "+v"(b1v[7]),
                 "+v"(b1v[8]), "+v"(b1v[9]), "+v"(b1v[10]), "+v"(b1v[11]));
    DRAIN();
    __syncthreads();
    int tra[4];
    {
        const int q4 = (lane >> 2) & 3, pi = lane & 3, row = 4 * g + q4;
#pragma unroll
        for (int u = 0; u < 4; ++u) tra[u] = row * 128 + ((((u + w) & 3) ^ ((row >> 1) & 3)) * 32) + pi * 8;
    }
    f32x4 acc[3][4], gram;
    v4u b[2][4];
    auto rd = [&](int ks, v4u(&o)[4]) {
#pragma unroll
        for (int u = 0; u < 4; ++u) {
            const v2u lo = lds_tr(L, tra[u] + ks * 4096), hi = lds_tr(L, tra[u] + ks * 4096 + 2048);
            o[u] = v4u{lo.x, lo.y, hi.x, hi.y};
        }
    };
    rd(0, b[0]);
    const unsigned voffr = lane * 16;
    static_for<0, 20>([&](auto kc) {
        constexpr int ks = kc.value;
        if constexpr (ks + 1 < 20) rd(ks + 1, b[(ks + 1) & 1]);
        static_for<0, 3>([&](auto rc) {
            constexpr int r = rc.value;
            static_for<0, 4>([&](auto uc) {
                constexpr int u = uc.value;
                if constexpr (ks == 0) mf_ra0<ks * 3 + r>(acc[r][u], b[ks & 1][u]);
                else mf_ra<ks * 3 + r>(acc[r][u], b[ks & 1][u]);
            });
            ring_ld_q<ks * 3 + r>(nxt, voffr);
        });
        if constexpr (ks == 0) {
            v4u m = b[0][0];
            if (g == 0) m.x &= 0x0000FFFFu;
            asm volatile("s_nop 1\n\t" MFMA_OP "%0, %1, %1, 0" : "=&v"(gram) : "v"(m));
        } else {
            asm volatile(MFMA_OP "%0, %1, %1, %0" : "+v"(gram) : "v"(b[ks & 1][0]));
        }
        __builtin_amdgcn_sched_barrier(0);
    });
    asm volatile("s_nop 7" : "+v"(gram), "+v"(acc[0][0]), "+v"(acc[0][1]), "+v"(acc[0][2]), "+v"(acc[0][3]), "+v"(acc[1][0]), "+v"(acc[1][1]),
                 "+v"(acc[1][2]), "+v"(acc[1][3]), "+v"(acc[2][0]), "+v"(acc[2][1]), "+v"(acc[2][2]), "+v"(acc[2][3]));
    {
        const int r = n & 3;
        const float d = r == 0 ? gram[0] : r == 1 ? gram[1] : r == 2 ? gram[2] : gram[3];
        if (g == (n >> 2)) ((LAS float *)(L + L_SS))[16 * w + n] = d;
    }
    if (!LAST) {
#pragma unroll
        for (int rd_ = 0; rd_ < 3; ++rd_) {
            int it = (rd_ * 4 + w) * 64 + lane;
            if (it >= 640) it = lane;
            const int row = 80 * j + (it >> 3), ch = it & 7;
            resid[rd_] = lds_rd128(L, row * 128 + (((ch >> 1) ^ ((row >> 1) & 3)) * 32) + (ch & 1) * 16);
        }
    }
    __syncthreads();
    float rstd[4];
#pragma unroll
    for (int u = 0; u < 4; ++u)
        rstd[u] = __builtin_amdgcn_rsqf(((LAS float *)(L + L_SS))[16 * ((u + w) & 3) + n] * (1.0f / 640.0f) + 1e-5f);
    const unsigned hoff = OFF_H + (LAST ? 1 : 0) * H_BYTES + i * (48 * 4096);
    v2u hp[3][4];
#pragma unroll
    for (int r = 0; r < 3; ++r)
#pragma unroll
        for (int u = 0; u < 4; ++u) {
            float y[4];
#pragma unroll
            for (int e = 0; e < 4; ++e) y[e] = gelu_f(acc[r][u][e] * rstd[u] + b1v[4 * r + e]);
            hp[r][u] = v2u{pk_bf(y[0], y[1]), pk_bf(y[2], y[3])};
        }
    const int ft0 = 12 * j + 3 * w;
    if ((w & 1) == 0) {
#pragma unroll
        for (int u = 0; u < 4; ++u) {
            const int tt = (u + w) & 3;
            st_sc1(v4u{hp[0][u].x, hp[0][u].y, hp[1][u].x, hp[1][u].y}, rs, lane * 16, hoff + (ft0 >> 1) * 4096 + tt * 1024);
            __builtin_amdgcn_raw_buffer_store_b64(hp[2][u], rs, lane * 16, hoff + ((ft0 + 2) >> 1) * 4096 + tt * 1024, AUX_SC1);
        }
    } else {
#pragma unroll
        for (int u = 0; u < 4; ++u) {
            const int tt = (u + w) & 3;
            __builtin_amdgcn_raw_buffer_store_b64(hp[0][u], rs, lane * 16 + 8, hoff + (ft0 >> 1) * 4096 + tt * 1024, AUX_SC1);
            st_sc1(v4u{hp[1][u].x, hp[1][u].y, hp[2][u].x, hp[2][u].y}, rs, lane * 16, hoff + ((ft0 + 1) >> 1) * 4096 + tt * 1024);
        }
    }
}

__device__ __forceinline__ void phase_r(LAS unsigned char *L, __amdgpu_buffer_rsrc_t rs, int j, int w, int lane, f32x4 (&acc)[5][4]) {
    const int g = lane >> 4;
    int tra[4];
    {
        const int q4 = (lane >> 2) & 3, pi = lane & 3, row = 4 * g + q4;
#pragma unroll
        for (int u = 0; u < 4; ++u) tra[u] = (5 * w) * 4096 + row * 128 + ((u ^ ((row >> 1) & 3)) * 32) + pi * 8;
    }
    v4u a[25];
#pragma unroll
    for (int f = 0; f < 25; ++f) a[f] = ld_pl(rs, lane * 16, OFF_RB2 + ((j * 4 + w) * 25 + f) * 1024);
#pragma unroll
    for (int ks = 0; ks < 5; ++ks) {
        v4u b[4];
#pragma unroll
        for (int u = 0; u < 4; ++u) {
            const v2u lo = lds_tr(L, tra[u] + ks * 4096), hi = lds_tr(L, tra[u] + ks * 4096 + 2048);
            b[u] = v4u{lo.x, lo.y, hi.x, hi.y};
        }
#pragma unroll
        for (int m = 0; m < 5; ++m)
#pragma unroll
            for (int u = 0; u < 4; ++u) {
                if (ks == 0) mf_vv0(acc[m][u], a[ks * 5 + m], b[u]);
                else mf_vv(acc[m][u], a[ks * 5 + m], b[u]);
            }
    }
}

template <bool LAST>
__device__ __forceinline__ void phase_g2(const Params &p, LAS unsigned char *L, __amdgpu_buffer_rsrc_t rs, int i, int j, int w, int lane,
                                         f32x4 (&acc)[5][4], const v4u (&resid)[3]) {
    const int g = lane >> 4, n = lane & 15;
    const unsigned hoff = OFF_H + (LAST ? 1 : 0) * H_BYTES + i * (48 * 4096) + (12 * w) * 4096;
    v4u h[3][4];
#pragma unroll
    for (int u = 0; u < 4; ++u) {
        h[0][u] = ld_sc1(rs, lane * 16, hoff + u * 1024);
        h[1][u] = ld_sc1(rs, lane * 16, hoff + 4096 + u * 1024);
    }
    static_for<0, 12>([&](auto kc) {
        constexpr int ks = kc.value;
        if constexpr (ks + 2 < 12) {
#pragma unroll
            for (int u = 0; u < 4; ++u) h[(ks + 2) % 3][u] = ld_sc1(rs, lane * 16, hoff + (ks + 2) * 4096 + u * 1024);
        }
        static_for<0, 5>([&](auto mc) {
            constexpr int m = mc.value;
            static_for<0, 4>([&](auto uc) {
                constexpr int u = uc.value;
                if constexpr (LAST) mf_ra<ks * 5 + m>(acc[m][u], h[ks % 3][u]);
                else if constexpr (ks == 0) mf_rb0<ks * 5 + m>(acc[m][u], h[ks % 3][u]);
                else mf_rb<ks * 5 + m>(acc[m][u], h[ks % 3][u]);
            });
        });
        __builtin_amdgcn_sched_barrier(0);
    });
    asm volatile("s_nop 7" : "+v"(acc[0][0]), "+v"(acc[0][1]), "+v"(acc[0][2]), "+v"(acc[0][3]), "+v"(acc[1][0]), "+v"(acc[1][1]), "+v"(acc[1][2]),
                 "+v"(acc[1][3]), "+v"(acc[2][0]), "+v"(acc[2][1]), "+v"(acc[2][2]), "+v"(acc[2][3]));
    asm volatile("s_nop 7" : "+v"(acc[3][0]), "+v"(acc[3][1]), "+v"(acc[3][2]), "+v"(acc[3][3]), "+v"(acc[4][0]), "+v"(acc[4][1]), "+v"(acc[4][2]),
                 "+v"(acc[4][3]));
    if (!LAST) {
#pragma unroll
        for (int m = 0; m < 5; ++m)
#pragma unroll
            for (int u = 0; u < 4; ++u) *(LAS f32x4 *)(L + w * L_PART + (16 * m + n) * 272 + (16 * u + 4 * g) * 4) = acc[m][u];
        __syncthreads();
        const unsigned xout = OFF_XH + 2 * XH_BYTES;
        float ssq[8];
#pragma unroll
        for (int e = 0; e < 8; ++e) ssq[e] = 0.f;
#pragma unroll
        for (int rd_ = 0; rd_ < 3; ++rd_) {
            const int it = (rd_ * 4 + w) * 64 + lane;
            if (rd_ < 2 || w < 2) {
                const int row = it >> 3, ch = it & 7;
                f32x4 s0 = {0.f, 0.f, 0.f, 0.f}, s1 = {0.f, 0.f, 0.f, 0.f};
#pragma unroll
                for (int ww = 0; ww < 4; ++ww) {
                    s0 += *(LAS f32x4 *)(L + ww * L_PART + row * 272 + ch * 32);
                    s1 += *(LAS f32x4 *)(L + ww * L_PART + row * 272 + ch * 32 + 16);
                }
                const int kp = 80 * j + row;
                const float bb = *(const GAS float *)((const GAS unsigned char *)p.ws + OFF_B2P + kp * 4);
                float v[8];
#pragma unroll
                for (int e = 0; e < 8; ++e) {
                    const unsigned u = resid[rd_][e >> 1];
                    v[e] = ((e & 1) ? bf_hi(u) : bf_lo(u)) + (e < 4 ? s0[e & 3] : s1[e & 3]) + bb;
                }
                const v4u o = {pk_bf(v[0], v[1]), pk_bf(v[2], v[3]), pk_bf(v[4], v[5]), pk_bf(v[6], v[7])};
                st_sc1(o, rs, kp * 4096 + i * 128 + ch * 16, xout);
                if (kp != 1) {
#pragma unroll
                    for (int e = 0; e < 8; ++e) {
                        const float z = (e & 1) ? bf_hi(o[e >> 1]) : bf_lo(o[e >> 1]);
                        ssq[e] += z * z;
                    }
                }
            }
        }
#pragma unroll
        for (int e = 0; e < 8; ++e) {
            ssq[e] += __shfl_xor(ssq[e], 8);
            ssq[e] += __shfl_xor(ssq[e], 16);
            ssq[e] += __shfl_xor(ssq[e], 32);
        }
        if (lane < 8) {
            *(LAS f32x4 *)(L + L_SSP + w * 256 + lane * 32) = f32x4{ssq[0], ssq[1], ssq[2], ssq[3]};
            *(LAS f32x4 *)(L + L_SSP + w * 256 + lane * 32 + 16) = f32x4{ssq[4], ssq[5], ssq[6], ssq[7]};
        }
        __syncthreads();
        if (w == 0) {
            const LAS float *sp = (const LAS float *)(L + L_SSP);
            const float t = (sp[lane] + sp[64 + lane]) + (sp[128 + lane] + sp[192 + lane]);
            __hip_atomic_fetch_add((GAS double *)((GAS unsigned char *)p.ws + OFF_SS + 16384) + 64 * i + lane, (double)t, RLX_AGENT);
        }
    } else {
#pragma unroll
        for (int m = 0; m < 5; ++m)
#pragma unroll
            for (int u = 0; u < 4; ++u) *(LAS f32x4 *)(L + w * L_PART + (16 * u + n) * 336 + (16 * m + 4 * g) * 4) = acc[m][u];
        __syncthreads();
#pragma unroll
        for (int rd_ = 0; rd_ < 3; ++rd_) {
            const int it = (rd_ * 4 + w) * 64 + lane;
            if (rd_ < 2 || w < 2) {
                const int tok = it / 10, ch = it - tok * 10;
                f32x4 s0 = {0.f, 0.f, 0.f, 0.f}, s1 = {0.f, 0.f, 0.f, 0.f};
#pragma unroll
                for (int ww = 0; ww < 4; ++ww) {
                    s0 += *(LAS f32x4 *)(L + ww * L_PART + tok * 336 + ch * 32);
                    s1 += *(LAS f32x4 *)(L + ww * L_PART + tok * 336 + ch * 32 + 16);
                }
                const int d0 = 80 * j + 8 * ch;
                const v4u bb = *(const GAS v4u *)((const GAS unsigned char *)p.b2 + d0 * 2);
                float v[8];
#pragma unroll
                for (int e = 0; e < 8; ++e) v[e] = (e < 4 ? s0[e & 3] : s1[e & 3]) + ((e & 1) ? bf_hi(bb[e >> 1]) : bf_lo(bb[e >> 1]));
                const v4u o = {pk_bf(v[0], v[1]), pk_bf(v[2], v[3]), pk_bf(v[4], v[5]), pk_bf(v[6], v[7])};
                *(GAS v4u *)((GAS unsigned char *)p.out + pos2tok(64 * i + tok) * 1280 + d0 * 2) = o;
            }
        }
    }
}

template <int PB, int PE> __global__ void __launch_bounds__(256, 1) k_main(Params p) {
    extern __shared__ __attribute__((aligned(16))) unsigned char smem[];
    LAS unsigned char *L = (LAS unsigned char *)smem;
    asm volatile("" ::: "a239");
    const __amdgpu_buffer_rsrc_t rs = __builtin_amdgcn_make_buffer_rsrc((void *)p.ws, 0, (int)WS_USED, RSRC_W3);
    const int lane = threadIdx.x & 63, w = __builtin_amdgcn_readfirstlane(threadIdx.x >> 6);
    const int bid = blockIdx.x, slot = bid >> 3;
    const int i = 4 * (bid & 7) + (slot >> 3), j = slot & 7;
    gu32 *ctl = (gu32 *)(p.ws + OFF_CTRL);
    const GAS unsigned char *wsg = (const GAS unsigned char *)p.ws;
    const GAS unsigned char *run1 = wsg + OFF_W1F + (j * 4 + w) * 61440;
    const GAS unsigned char *run2 = wsg + OFF_W2F + (j * 4 + w) * 61440;
    const GAS unsigned char *run3 = wsg + OFF_W2T + (j * 4 + w) * 61440;
    const unsigned voffr = lane * 16;
    const bool isC = bid < 160;

    if constexpr (PB == 0) {
        if (isC) {
            CConst K;
            c_consts(K, w, lane);
            phase_c<false>(K, L, rs, bid, 0, w, lane);
        }
        if constexpr (PE > 1) {
            if (isC) signal_add(ctl + CW_BAR + (0 * 16 + (bid & 15)) * 64);
        }
    }
    if constexpr (PB <= 1 && PE > 1) {
        ring_fill<0, 60>(run1, voffr);
        if constexpr (PB == 0) {
            if (!wait_words<16>(ctl + CW_BAR + 0 * 16 * 64, 10u, ctl + CW_TMO, L)) return;
        }
        v4u resid[3];
        phase_g1<false>(p, L, rs, i, j, w, lane, run2, resid);
        signal_add(ctl + CW_GRP + i * 64);
        if (!wait_words<1>(ctl + CW_GRP + i * 64, 8u, ctl + CW_TMO, L)) return;
        f32x4 acc[5][4];
        phase_g2<false>(p, L, rs, i, j, w, lane, acc, resid);
        if constexpr (PE > 2) signal_add(ctl + CW_BAR + (1 * 16 + (bid & 15)) * 64);
    }
    if constexpr (PB <= 2 && PE > 2) {
        if (isC) {
            CConst K;
            c_consts(K, w, lane);
            if constexpr (PB < 2) {
                if (!wait_words<16>(ctl + CW_BAR + 1 * 16 * 64, 16u, ctl + CW_TMO, L)) return;
            }
            phase_c<true>(K, L, rs, bid, 1, w, lane);
            if constexpr (PE > 3) signal_add(ctl + CW_BAR + (2 * 16 + (bid & 15)) * 64);
        }
    }
    if constexpr (PE > 3) {
        ring_fill<0, 60>(run1, voffr);
        if constexpr (PB < 3) {
            if (!wait_words<16>(ctl + CW_BAR + 2 * 16 * 64, 10u, ctl + CW_TMO, L)) return;
        }
        v4u resid[3];
        phase_g1<true>(p, L, rs, i, j, w, lane, run3, resid);
        signal_add(ctl + CW_GRP + (32 + i) * 64);
        f32x4 acc[5][4];
        phase_r(L, rs, j, w, lane, acc);
        if (!wait_words<1>(ctl + CW_GRP + (32 + i) * 64, 8u, ctl + CW_TMO, L)) return;
        phase_g2<true>(p, L, rs, i, j, w, lane, acc, resid);
    }
}

__device__ __forceinline__ float bfv(const uint16_t *a, int idx) { return __builtin_bit_cast(float, (unsigned)a[idx] << 16); }
__device__ __forceinline__ uint16_t f2bf(float f) {
    unsigned u = __builtin_bit_cast(unsigned, f);
    u += 0x7FFFu + ((u >> 16) & 1u);
    return (uint16_t)(u >> 16);
}
__device__ __forceinline__ float r_elem(const float *ct, int d, int kp) {
    if (kp == 0) return ct[(320 * d) % 640] * 0.039528471f;
    if (kp == 1) return 0.039528471f;
    const int k = kp >> 1;
    return ct[(k * d + ((kp & 1) ? 160 : 0)) % 640] * 0.055901699f;
}
__device__ __forceinline__ void fill_ct(float *ct) {
    for (int m = threadIdx.x; m < 640; m += blockDim.x) ct[m] = (float)cospi((double)m / 320.0);
    __syncthreads();
}
__global__ void k_prep_xh0(Params p) {
    __shared__ float ct[640];
    fill_ct(ct);
    const int kp = blockIdx.x;
    uint16_t *o = (uint16_t *)(p.ws + OFF_XH) + (size_t)kp * 2048;
    for (int e = 0; e < 8; ++e) {
        const int pos = threadIdx.x + 256 * e, s = pos2tok(pos);
        float a = 0.f;
        for (int d = 0; d < 640; ++d) a += bfv(p.x, s * 640 + d) * r_elem(ct, d, kp);
        o[pos] = f2bf(a);
    }
}
__global__ void k_prep_misc(Params p) {
    __shared__ float ct[640];
    fill_ct(ct);
    const int t = blockIdx.x * 256 + threadIdx.x;
    const int s = pos2tok(t);
    float m = 0.f;
    for (int d = 0; d < 640; ++d) m += bfv(p.x, s * 640 + d);
    m *= (1.0f / 640.0f);
    float v = 0.f;
    for (int d = 0; d < 640; ++d) { const float z = bfv(p.x, s * 640 + d) - m; v += z * z; }
    double *ss = (double *)(p.ws + OFF_SS);
    ss[t] = (double)v;
    ss[2048 + t] = 0.0;
    unsigned *cw = (unsigned *)(p.ws + OFF_CTRL);
    for (int e = t; e < 16384; e += 2048) cw[e] = 0u;
    if (t < 640) {
        float a = 0.f;
        for (int d = 0; d < 640; ++d) a += bfv(p.b2, d) * r_elem(ct, d, t);
        ((float *)(p.ws + OFF_B2P))[t] = a;
    }
}
__global__ void k_prep_w1f(Params p) {
    __shared__ float ct[640];
    fill_ct(ct);
    const int e = blockIdx.x * 256 + threadIdx.x;
    const int jj = e & 7, lane = (e >> 3) & 63, fq = e >> 9, q = fq % 60, jw = fq / 60, w = jw & 3, j = jw >> 2;
    const int ks = q / 3, r = q % 3, g = lane >> 4, n = lane & 15;
    const int f = 192 * j + 48 * w + 16 * r + n, kp = 32 * ks + 16 * (jj >> 2) + 4 * g + (jj & 3);
    float a = 0.f;
    if (kp != 1)
        for (int d = 0; d < 640; ++d) a += r_elem(ct, d, kp) * bfv(p.w1, d * 1536 + f);
    ((uint16_t *)(p.ws + OFF_W1F))[e] = f2bf(a);
}
__global__ void k_prep_w2f(Params p) {
    __shared__ float ct[640];
    fill_ct(ct);
    const int e = blockIdx.x * 256 + threadIdx.x;
    const int jj = e & 7, lane = (e >> 3) & 63, fq = e >> 9, q = fq % 60, jw = fq / 60, w = jw & 3, j = jw >> 2;
    const int ks = q / 5, m = q % 5, g = lane >> 4, n = lane & 15;
    const int f = 384 * w + 32 * ks + 16 * (jj >> 2) + 4 * g + (jj & 3), kp = 80 * j + 16 * m + n;
    float a = 0.f;
    for (int d = 0; d < 640; ++d) a += bfv(p.w2, f * 640 + d) * r_elem(ct, d, kp);
    ((uint16_t *)(p.ws + OFF_W2F))[e] = f2bf(a);
    ((uint16_t *)(p.ws + OFF_W2T))[e] = p.w2[f * 640 + kp];
}
__global__ void k_prep_rb2(Params p) {
    __shared__ float ct[640];
    fill_ct(ct);
    const int e = blockIdx.x * 256 + threadIdx.x;
    const int jj = e & 7, lane = (e >> 3) & 63, fq = e >> 9, q = fq % 25, jw = fq / 25, w = jw & 3, j = jw >> 2;
    const int ks = 5 * w + q / 5, m = q % 5, g = lane >> 4, n = lane & 15;
    const int d = 80 * j + 16 * m + n, kp = 32 * ks + 16 * (jj >> 2) + 4 * g + (jj & 3);
    ((uint16_t *)(p.ws + OFF_RB2))[e] = f2bf(r_elem(ct, d, kp));
}
__global__ void k_prep_wt(Params p) {
    const int e = blockIdx.x * 256 + threadIdx.x;
    const int jj = e & 7, lane = (e >> 3) & 63, w = (e >> 9) & 3, part = (e >> 11) & 1, col = e >> 12;
    const int g = lane >> 4, cl = lane & 15, td = jj >> 2, r = jj & 3;
    const int jf = 16 * w + cl + 64 * (16 * td + 4 * g + r), ci = col == 0 ? 320 : col;
    const float v = bfv(part ? p.wi : p.wr, jf * 321 + ci) * (1.0f / 2048.0f);
    ((uint16_t *)(p.ws + OFF_WT))[e] = f2bf(v);
}

static void fill_params(Params &p, void *const *d_in, void *d_out, void *d_ws) {
    p.x = (const uint16_t *)d_in[0];
    p.wr = (const uint16_t *)d_in[1];
    p.wi = (const uint16_t *)d_in[2];
    p.w1 = (const uint16_t *)d_in[4];
    p.b1 = (const uint16_t *)d_in[5];
    p.w2 = (const uint16_t *)d_in[6];
    p.b2 = (const uint16_t *)d_in[7];
    p.out = (uint16_t *)d_out;
    p.ws = (unsigned char *)d_ws;
}
static void launch_prep(const Params &p, hipStream_t stream) {
    k_prep_misc<<<8, 256, 0, stream>>>(p);
    k_prep_xh0<<<640, 256, 0, stream>>>(p);
    k_prep_w1f<<<3840, 256, 0, stream>>>(p);
    k_prep_w2f<<<3840, 256, 0, stream>>>(p);
    k_prep_rb2<<<1600, 256, 0, stream>>>(p);
    k_prep_wt<<<5120, 256, 0, stream>>>(p);
}
template <int PB, int PE> static void launch_main(const Params &p, hipStream_t stream) {
    hipFuncSetAttribute((const void *)k_main<PB, PE>, hipFuncAttributeMaxDynamicSharedMemorySize, L_BYTES);
    k_main<PB, PE><<<256, 256, L_BYTES, stream>>>(p);
}

extern "C" void kernel_launch(void *const *d_in, const int *in_sizes, int n_in, void *d_out, int out_size, void *d_ws, size_t ws_size,
                              hipStream_t stream) {
    Params p;
    fill_params(p, d_in, d_out, d_ws);
    launch_prep(p, stream);
    launch_main<0, 4>(p, stream);
}
```

```cpp
#include <hip/hip_runtime.h>
#include <stdint.h>
#include <type_traits>

#define GAS __attribute__((address_space(1)))
#define LAS __attribute__((address_space(3)))
typedef unsigned v4u __attribute__((ext_vector_type(4)));
typedef unsigned v2u __attribute__((ext_vector_type(2)));
typedef float f32x4 __attribute__((ext_vector_type(4)));
typedef float f32x2 __attribute__((ext_vector_type(2)));
typedef short s16x4 __attribute__((ext_vector_type(4)));
typedef __bf16 bf16x2 __attribute__((ext_vector_type(2)));
typedef __bf16 bf16x8 __attribute__((ext_vector_type(8)));
typedef GAS unsigned gu32;
#define RLX_AGENT __ATOMIC_RELAXED, __HIP_MEMORY_SCOPE_AGENT

constexpr int S_ = 2048, D_ = 640, F_ = 1536;
constexpr int AUX_SC1 = 16;
constexpr int RSRC_W3 = 0x00020000;

constexpr unsigned OFF_CTRL = 0;
constexpr unsigned OFF_SS = 65536;
constexpr unsigned OFF_B2P = OFF_SS + 32768;
constexpr unsigned XH_BYTES = D_ * S_ * 2;
constexpr unsigned OFF_XH = 131072;
constexpr unsigned H_BYTES = S_ * F_ * 2;
constexpr unsigned OFF_H = OFF_XH + 4 * XH_BYTES;
constexpr unsigned WF_BYTES = D_ * F_ * 2;
constexpr unsigned OFF_W1F = OFF_H + 2 * H_BYTES;
constexpr unsigned OFF_W2F = OFF_W1F + WF_BYTES;
constexpr unsigned OFF_W2T = OFF_W2F + WF_BYTES;
constexpr unsigned OFF_RB2 = OFF_W2T + WF_BYTES;
constexpr unsigned OFF_WT = OFF_RB2 + D_ * D_ * 2;
constexpr unsigned WS_USED = OFF_WT + 320 * 8192;

constexpr int CW_TMO = 0, CW_BAR = 1024, CW_GRP = 8192;
constexpr unsigned SPIN_CAP = 1u << 16;

constexpr int L_TILE = 0;
constexpr int L_PART = 21760;
constexpr int L_MISC = 4 * L_PART;
constexpr int L_SS = L_MISC;
constexpr int L_SSP = L_MISC + 256;
constexpr int L_FLAG = L_MISC + 256 + 1024;
constexpr int L_TS = L_MISC + 2048;
constexpr int L_BYTES = L_MISC + 2048 + 256;
constexpr unsigned OFF_TS = 200u << 20;
#define TS(n)
#define TS_DUMP()

template <int I, int N, class Fn> __device__ __forceinline__ void static_for(Fn &&f) {
    if constexpr (I < N) {
        f(std::integral_constant<int, I>{});
        static_for<I + 1, N>(f);
    }
}

__device__ __forceinline__ float bf_lo(unsigned u) { return __builtin_bit_cast(float, u << 16); }
__device__ __forceinline__ float bf_hi(unsigned u) { return __builtin_bit_cast(float, u & 0xFFFF0000u); }
__device__ __forceinline__ unsigned pk_bf(float lo, float hi) {
    f32x2 v = {lo, hi};
    return __builtin_bit_cast(unsigned, __builtin_convertvector(v, bf16x2));
}
__host__ __device__ __forceinline__ int pos2tok(int p) {
    const int l = p >> 3, t = (p >> 2) & 1, r = p & 3;
    const int w = l >> 6, g = (l >> 4) & 3, bl = l & 15;
    return 32 * (16 * w + 4 * g + r) + 16 * t + bl;
}

#define MFMA_OP "v_mfma_f32_16x16x32_bf16 "
template <int Q> __device__ __forceinline__ void mf_ra0(f32x4 &c, v4u b) {
    asm volatile(MFMA_OP "%0, a[%c2:%c3], %1, 0" : "=&v"(c) : "v"(b), "i"(4 * Q), "i"(4 * Q + 3));
}
template <int Q> __device__ __forceinline__ void mf_ra(f32x4 &c, v4u b) {
    asm volatile(MFMA_OP "%0, a[%c2:%c3], %1, %0" : "+v"(c) : "v"(b), "i"(4 * Q), "i"(4 * Q + 3));
}
template <int Q> __device__ __forceinline__ void mf_rb0(f32x4 &c, v4u a) {
    asm volatile(MFMA_OP "%0, %1, a[%c2:%c3], 0" : "=&v"(c) : "v"(a), "i"(4 * Q), "i"(4 * Q + 3));
}
template <int Q> __device__ __forceinline__ void mf_rb(f32x4 &c, v4u a) {
    asm volatile(MFMA_OP "%0, %1, a[%c2:%c3], %0" : "+v"(c) : "v"(a), "i"(4 * Q), "i"(4 * Q + 3));
}
__device__ __forceinline__ void mf_vv0(f32x4 &c, v4u a, v4u b) {
    asm volatile(MFMA_OP "%0, %1, %2, 0" : "=&v"(c) : "v"(a), "v"(b));
}
__device__ __forceinline__ void mf_vv(f32x4 &c, v4u a, v4u b) {
    asm volatile(MFMA_OP "%0, %1, %2, %0" : "+v"(c) : "v"(a), "v"(b));
}
__device__ __forceinline__ void mf_cplx2(f32x4 &c, v4u a0, v4u b0, v4u a1, v4u b1) {
    asm volatile("s_nop 1\n\t" MFMA_OP "%0, %1, %2, 0\n\t" MFMA_OP "%0, %3, %4, %0"
                 : "=&v"(c) : "v"(a0), "v"(b0), "v"(a1), "v"(b1));
}
__device__ __forceinline__ void mf_cplx4(f32x4 &c, v4u a0, v4u b0, v4u a1, v4u b1, v4u a2, v4u b2, v4u a3, v4u b3) {
    asm volatile("s_nop 1\n\t" MFMA_OP "%0, %1, %2, 0\n\t" MFMA_OP "%0, %3, %4, %0\n\t" MFMA_OP "%0, %5, %6, %0\n\t" MFMA_OP "%0, %7, %8, %0"
                 : "=&v"(c) : "v"(a0), "v"(b0), "v"(a1), "v"(b1), "v"(a2), "v"(b2), "v"(a3), "v"(b3));
}
#define MF_FENCE4(a, b, c, d) asm volatile("s_nop 7" : "+v"(a), "+v"(b), "+v"(c), "+v"(d))

template <int Q, int IMM> __device__ __forceinline__ void ring_ld(const GAS unsigned char *sbase, unsigned voff) {
    asm volatile("global_load_dwordx4 a[%c2:%c3], %0, %1 offset:%c4" ::"v"(voff), "s"(sbase), "i"(4 * Q), "i"(4 * Q + 3), "i"(IMM));
}
template <int Q> __device__ __forceinline__ void ring_ld_q(const GAS unsigned char *run, unsigned voff) {
    ring_ld<Q, (Q & 3) * 1024>(run + (Q >> 2) * 4096, voff);
}
template <int LO, int HI> __device__ __forceinline__ void ring_fill(const GAS unsigned char *run, unsigned voff) {
    static_for<LO, HI>([&](auto q) { ring_ld_q<q.value>(run, voff); });
}
#define DRAIN() asm volatile("s_waitcnt vmcnt(0)" ::: "memory")

__device__ __forceinline__ v4u ld_sc1(__amdgpu_buffer_rsrc_t rs, int voff, int soff) {
    return __builtin_amdgcn_raw_buffer_load_b128(rs, voff, soff, AUX_SC1);
}
__device__ __forceinline__ v4u ld_pl(__amdgpu_buffer_rsrc_t rs, int voff, int soff) {
    return __builtin_amdgcn_raw_buffer_load_b128(rs, voff, soff, 0);
}
__device__ __forceinline__ void st_sc1(v4u v, __amdgpu_buffer_rsrc_t rs, int voff, int soff) {
    __builtin_amdgcn_raw_buffer_store_b128(v, rs, voff, soff, AUX_SC1);
}
__device__ __forceinline__ v4u lds_rd128(LAS unsigned char *L, int off) { return *(LAS v4u *)(L + off); }
__device__ __forceinline__ v2u lds_tr(LAS unsigned char *L, int off) {
    return __builtin_bit_cast(v2u, __builtin_amdgcn_ds_read_tr16_b64_v4i16((LAS s16x4 *)(L + off)));
}

struct Params {
    const uint16_t *x, *wr, *wi, *w1, *b1, *w2, *b2;
    uint16_t *out;
    unsigned char *ws;
};

__device__ __forceinline__ void signal_add(gu32 *word) {
    DRAIN();
    __syncthreads();
    if (threadIdx.x == 0) __hip_atomic_fetch_add(word, 1u, RLX_AGENT);
}
template <int NSH> __device__ __forceinline__ bool wait_words(gu32 *words, unsigned want, gu32 *tmo, LAS unsigned char *L) {
    volatile LAS unsigned *flag = (volatile LAS unsigned *)(L + L_FLAG);
    if (threadIdx.x < 64) {
        const int lane = threadIdx.x;
        unsigned ok = 1u;
        for (unsigned spins = 0;;) {
            unsigned v = want;
            if (lane < NSH) v = __hip_atomic_load(words + lane * 64, RLX_AGENT);
            if (__all(v >= want)) break;
            if (++spins > SPIN_CAP) {
                if (lane == 0) __hip_atomic_store(tmo, 1u, RLX_AGENT);
                ok = 0u;
                break;
            }
            if ((spins & 255u) == 0u && __hip_atomic_load(tmo, RLX_AGENT) != 0u) { ok = 0u; break; }
        }
        __builtin_amdgcn_fence(__ATOMIC_ACQUIRE, "wavefront");
        if (lane == 0) *flag = ok;
    }
    __syncthreads();
    return *flag != 0u;
}

struct CConst {
    v4u Cr[2], Ci[2], nCi[2];
    v4u Er[2], Ei[2], nEi[2];
    float t1r[8], t1i[8], t2r[8], t2i[8];
};
__device__ __forceinline__ void c_consts(CConst &K, int w, int lane) {
    const int g = lane >> 4, i = lane & 15;
#pragma unroll
    for (int ks = 0; ks < 2; ++ks) {
        float c[8], s[8];
#pragma unroll
        for (int j = 0; j < 8; ++j) {
            const float r = (float)(((32 * ks + 8 * g + j) * (16 * w + i)) & 63) * (1.0f / 64.0f);
            c[j] = __builtin_amdgcn_cosf(r);
            s[j] = __builtin_amdgcn_sinf(r);
        }
#pragma unroll
        for (int h = 0; h < 4; ++h) {
            K.Cr[ks][h] = pk_bf(c[2 * h], c[2 * h + 1]);
            K.nCi[ks][h] = pk_bf(s[2 * h], s[2 * h + 1]);
            K.Ci[ks][h] = K.nCi[ks][h] ^ 0x80008000u;
        }
    }
#pragma unroll
    for (int td = 0; td < 2; ++td) {
        float c[8], s[8];
#pragma unroll
        for (int j = 0; j < 8; ++j) {
            const float r = (float)(((16 * (j >> 2) + 4 * g + (j & 3)) * (16 * td + i)) & 31) * (1.0f / 32.0f);
            c[j] = __builtin_amdgcn_cosf(r);
            s[j] = __builtin_amdgcn_sinf(r);
        }
#pragma unroll
        for (int h = 0; h < 4; ++h) {
            K.Er[td][h] = pk_bf(c[2 * h], c[2 * h + 1]);
            K.nEi[td][h] = pk_bf(s[2 * h], s[2 * h + 1]);
            K.Ei[td][h] = K.nEi[td][h] ^ 0x80008000u;
        }
    }
#pragma unroll
    for (int e = 0; e < 8; ++e) {
        const int tb = e >> 2, r = e & 3;
        const float a1 = (float)(((16 * tb + 4 * g + r) * (16 * w + i)) & 2047) * (1.0f / 2048.0f);
        K.t1r[e] = __builtin_amdgcn_cosf(a1);
        K.t1i[e] = -__builtin_amdgcn_sinf(a1);
        const float a2 = (float)(((16 * tb + i) * (16 * w + 4 * g + r)) & 2047) * (1.0f / 2048.0f);
        K.t2r[e] = __builtin_amdgcn_cosf(a2) * (1.0f / 2048.0f);
        K.t2i[e] = __builtin_amdgcn_sinf(a2) * (1.0f / 2048.0f);
    }
}

__device__ __forceinline__ void cmul_pack(const f32x4 (&re)[2], const f32x4 (&im)[2], const float (&tr)[8], const float (&ti)[8], v4u &ore, v4u &oim) {
    float a[8], b[8];
#pragma unroll
    for (int e = 0; e < 8; ++e) {
        const float x = re[e >> 2][e & 3], y = im[e >> 2][e & 3];
        a[e] = x * tr[e] - y * ti[e];
        b[e] = x * ti[e] + y * tr[e];
    }
#pragma unroll
    for (int h = 0; h < 4; ++h) {
        ore[h] = pk_bf(a[2 * h], a[2 * h + 1]);
        oim[h] = pk_bf(b[2 * h], b[2 * h + 1]);
    }
}

template <bool SC1>
__device__ __forceinline__ void phase_c(const CConst &K, LAS unsigned char *L, __amdgpu_buffer_rsrc_t rs, int q, int layer, int w, int lane) {
    const int g = lane >> 4, bl = lane & 15;
    const unsigned xin = OFF_XH + (2 * layer) * XH_BYTES, xout = OFF_XH + (2 * layer + 1) * XH_BYTES;
    const int voff = (64 * w + lane) * 16;
    v4u xr[2], xi[2], wr[2], wi[2], ssv[4];
#pragma unroll
    for (int c = 0; c < 2; ++c) {
        xr[c] = __builtin_amdgcn_raw_buffer_load_b128(rs, voff, xin + (4 * q + 2 * c) * 4096, SC1 ? AUX_SC1 : 0);
        xi[c] = __builtin_amdgcn_raw_buffer_load_b128(rs, voff, xin + (4 * q + 2 * c + 1) * 4096, SC1 ? AUX_SC1 : 0);
    }
#pragma unroll
    for (int h = 0; h < 4; ++h)
        ssv[h] = __builtin_amdgcn_raw_buffer_load_b128(rs, (64 * w + lane) * 64 + h * 16, OFF_SS + layer * 16384, SC1 ? AUX_SC1 : 0);
#pragma unroll
    for (int c = 0; c < 2; ++c) {
        wr[c] = ld_pl(rs, voff, OFF_WT + (2 * q + c) * 8192);
        wi[c] = ld_pl(rs, voff, OFF_WT + (2 * q + c) * 8192 + 4096);
    }
    float rstd[8];
#pragma unroll
    for (int e = 0; e < 8; ++e) {
        const v2u u = {ssv[e >> 1][(e & 1) * 2], ssv[e >> 1][(e & 1) * 2 + 1]};
        const float ss = (float)__builtin_bit_cast(double, u);
        rstd[e] = __builtin_amdgcn_rsqf(ss * (1.0f / 640.0f) + 1e-5f);
    }
    const int wadr = bl * 128 + (((2 * w + (g >> 1)) ^ ((bl >> 1) & 7)) * 16) + (g & 1) * 8;
    const int radr0 = bl * 128 + ((g ^ ((bl >> 1) & 7)) * 16), radr1 = bl * 128 + (((4 + g) ^ ((bl >> 1) & 7)) * 16);
    const bool col0 = (q == 0);
#pragma unroll
    for (int c = 0; c < 2; ++c) {
        float zr[8], zi[8];
#pragma unroll
        for (int h = 0; h < 4; ++h) {
            zr[2 * h] = bf_lo(xr[c][h]) * rstd[2 * h];
            zr[2 * h + 1] = bf_hi(xr[c][h]) * rstd[2 * h + 1];
            zi[2 * h] = bf_lo(xi[c][h]) * rstd[2 * h];
            zi[2 * h + 1] = bf_hi(xi[c][h]) * rstd[2 * h + 1];
        }
        if (c == 0 && col0) {
#pragma unroll
            for (int e = 0; e < 8; ++e) zi[e] = 0.f;
        }
#pragma unroll
        for (int t = 0; t < 2; ++t) {
            const v2u a = {pk_bf(zr[4 * t], zr[4 * t + 1]), pk_bf(zr[4 * t + 2], zr[4 * t + 3])};
            const v2u b = {pk_bf(zi[4 * t], zi[4 * t + 1]), pk_bf(zi[4 * t + 2], zi[4 * t + 3])};
            *(LAS v2u *)(L + c * 8192 + t * 2048 + wadr) = a;
            *(LAS v2u *)(L + c * 8192 + 4096 + t * 2048 + wadr) = b;
        }
    }
    __syncthreads();
#pragma unroll
    for (int c = 0; c < 2; ++c) {
        f32x4 ur[2], ui[2];
#pragma unroll
        for (int tb = 0; tb < 2; ++tb) {
            const v4u r0 = lds_rd128(L, c * 8192 + tb * 2048 + radr0), r1 = lds_rd128(L, c * 8192 + tb * 2048 + radr1);
            const v4u i0 = lds_rd128(L, c * 8192 + 4096 + tb * 2048 + radr0), i1 = lds_rd128(L, c * 8192 + 4096 + tb * 2048 + radr1);
            mf_cplx4(ur[tb], r0, K.Cr[0], r1, K.Cr[1], i0, K.nCi[0], i1, K.nCi[1]);
            mf_cplx4(ui[tb], r0, K.Ci[0], r1, K.Ci[1], i0, K.Cr[0], i1, K.Cr[1]);
        }
        MF_FENCE4(ur[0], ur[1], ui[0], ui[1]);
        v4u vr, vi;
        cmul_pack(ur, ui, K.t1r, K.t1i, vr, vi);
        f32x4 Xr[2], Xi[2];
#pragma unroll
        for (int td = 0; td < 2; ++td) {
            mf_cplx2(Xr[td], K.Er[td], vr, K.nEi[td], vi);
            mf_cplx2(Xi[td], K.Ei[td], vr, K.Er[td], vi);
        }
        MF_FENCE4(Xr[0], Xr[1], Xi[0], Xi[1]);
        float fr[8], fi[8];
#pragma unroll
        for (int h = 0; h < 4; ++h) {
            fr[2 * h] = bf_lo(wr[c][h]); fr[2 * h + 1] = bf_hi(wr[c][h]);
            fi[2 * h] = bf_lo(wi[c][h]); fi[2 * h + 1] = bf_hi(wi[c][h]);
        }
        v4u yr, yi;
        cmul_pack(Xr, Xi, fr, fi, yr, yi);
        f32x4 pr[2], pi[2];
#pragma unroll
        for (int tb = 0; tb < 2; ++tb) {
            mf_cplx2(pr[tb], yr, K.Er[tb], yi, K.Ei[tb]);
            mf_cplx2(pi[tb], yr, K.nEi[tb], yi, K.Er[tb]);
        }
        MF_FENCE4(pr[0], pr[1], pi[0], pi[1]);
        v4u pre, pim;
        cmul_pack(pr, pi, K.t2r, K.t2i, pre, pim);
#pragma unroll
        for (int t = 0; t < 2; ++t) {
            *(LAS v2u *)(L + 16384 + c * 8192 + t * 2048 + wadr) = v2u{pre[2 * t], pre[2 * t + 1]};
            *(LAS v2u *)(L + 16384 + c * 8192 + 4096 + t * 2048 + wadr) = v2u{pim[2 * t], pim[2 * t + 1]};
        }
    }
    __syncthreads();
#pragma unroll
    for (int c = 0; c < 2; ++c) {
        f32x4 or_[2], oi[2];
#pragma unroll
        for (int tb = 0; tb < 2; ++tb) {
            const int base = 16384 + c * 8192 + tb * 2048;
            const v4u r0 = lds_rd128(L, base + radr0), r1 = lds_rd128(L, base + radr1);
            const v4u i0 = lds_rd128(L, base + 4096 + radr0), i1 = lds_rd128(L, base + 4096 + radr1);
            mf_cplx4(or_[tb], K.Cr[0], r0, K.Cr[1], r1, K.Ci[0], i0, K.Ci[1], i1);
            mf_cplx4(oi[tb], K.Cr[0], i0, K.Cr[1], i1, K.nCi[0], r0, K.nCi[1], r1);
        }
        MF_FENCE4(or_[0], or_[1], oi[0], oi[1]);
        v4u orr, oii;
#pragma unroll
        for (int h = 0; h < 4; ++h) {
            const int t = h >> 1, r = (h & 1) * 2;
            orr[h] = pk_bf(bf_lo(xr[c][h]) + or_[t][r], bf_hi(xr[c][h]) + or_[t][r + 1]);
            oii[h] = pk_bf(bf_lo(xi[c][h]) + oi[t][r], bf_hi(xi[c][h]) + oi[t][r + 1]);
        }
        if (c == 0 && col0) oii = xi[0];
        st_sc1(orr, rs, voff, xout + (4 * q + 2 * c) * 4096);
        st_sc1(oii, rs, voff, xout + (4 * q + 2 * c + 1) * 4096);
    }
}

__device__ __forceinline__ float gelu_f(float x) {
    const float t = x * (x * x * -0.10294324f + -2.3022082f);
    return x * __builtin_amdgcn_rcpf(1.0f + __builtin_amdgcn_exp2f(t));
}

template <bool LAST>
__device__ __forceinline__ void phase_g1(const Params &p, LAS unsigned char *L, __amdgpu_buffer_rsrc_t rs, int i, int j, int w, int lane,
                                         const GAS unsigned char *nxt, v4u (&resid)[3]) {
    const int g = lane >> 4, n = lane & 15;
    const unsigned xin = OFF_XH + (LAST ? 3 : 1) * XH_BYTES;
    {
        const int row = 8 * w + (lane >> 3), ch = lane & 7;
        const int voff = row * 4096 + i * 128 + ch * 16;
        const int ladr = row * 128 + (((ch >> 1) ^ ((row >> 1) & 3)) * 32) + (ch & 1) * 16;
        v4u t[20];
#pragma unroll
        for (int ks = 0; ks < 20; ++ks) t[ks] = ld_sc1(rs, voff, xin + ks * 32 * 4096);
#pragma unroll
        for (int ks = 0; ks < 20; ++ks) *(LAS v4u *)(L + ks * 4096 + ladr) = t[ks];
    }
    float b1v[12];
#pragma unroll
    for (int r = 0; r < 3; ++r) {
        const v2u u = *(const GAS v2u *)((const GAS unsigned char *)p.b1 + (192 * j + 48 * w + 16 * r + 4 * g) * 2);
        b1v[4 * r] = bf_lo(u.x); b1v[4 * r + 1] = bf_hi(u.x); b1v[4 * r + 2] = bf_lo(u.y); b1v[4 * r + 3] = bf_hi(u.y);
    }
    asm volatile("" : "+v"(b1v[0]), "+v"(b1v[1]), "+v"(b1v[2]), "+v"(b1v[3]), "+v"(b1v[4]), "+v"(b1v[5]), "+v"(b1v[6]), "+v"(b1v[7]),
                 "+v"(b1v[8]), "+v"(b1v[9]), "+v"(b1v[10]), "+v"(b1v[11]));
    DRAIN();
    __syncthreads();
    TS(LAST ? 17 : 5);
    int tra[4];
    {
        const int q4 = (lane >> 2) & 3, pi = lane & 3, row = 4 * g + q4;
#pragma unroll
        for (int u = 0; u < 4; ++u) tra[u] = row * 128 + ((((u + w) & 3) ^ ((row >> 1) & 3)) * 32) + pi * 8;
    }
    f32x4 acc[3][4], gram;
    v4u b[2][4];
    auto rd = [&](int ks, v4u(&o)[4]) {
#pragma unroll
        for (int u = 0; u < 4; ++u) {
            const v2u lo = lds_tr(L, tra[u] + ks * 4096), hi = lds_tr(L, tra[u] + ks * 4096 + 2048);
            o[u] = v4u{lo.x, lo.y, hi.x, hi.y};
        }
    };
    rd(0, b[0]);
    const unsigned voffr = lane * 16;
    static_for<0, 20>([&](auto kc) {
        constexpr int ks = kc.value;
        if constexpr (ks + 1 < 20) rd(ks + 1, b[(ks + 1) & 1]);
        static_for<0, 3>([&](auto rc) {
            constexpr int r = rc.value;
            static_for<0, 4>([&](auto uc) {
                constexpr int u = uc.value;
                if constexpr (ks == 0) mf_ra0<ks * 3 + r>(acc[r][u], b[ks & 1][u]);
                else mf_ra<ks * 3 + r>(acc[r][u], b[ks & 1][u]);
            });
            ring_ld_q<ks * 3 + r>(nxt, voffr);
        });
        if constexpr (ks == 0) {
            v4u m = b[0][0];
            if (g == 0) m.x &= 0x0000FFFFu;
            asm volatile("s_nop 1\n\t" MFMA_OP "%0, %1, %1, 0" : "=&v"(gram) : "v"(m));
        } else {
            asm volatile(MFMA_OP "%0, %1, %1, %0" : "+v"(gram) : "v"(b[ks & 1][0]));
        }
        __builtin_amdgcn_sched_barrier(0);
    });
    asm volatile("s_nop 7" : "+v"(gram), "+v"(acc[0][0]), "+v"(acc[0][1]), "+v"(acc[0][2]), "+v"(acc[0][3]), "+v"(acc[1][0]), "+v"(acc[1][1]),
                 "+v"(acc[1][2]), "+v"(acc[1][3]), "+v"(acc[2][0]), "+v"(acc[2][1]), "+v"(acc[2][2]), "+v"(acc[2][3]));
    TS(LAST ? 18 : 6);
    {
        const int r = n & 3;
        const float d = r == 0 ? gram[0] : r == 1 ? gram[1] : r == 2 ? gram[2] : gram[3];
        if (g == (n >> 2)) ((LAS float *)(L + L_SS))[16 * w + n] = d;
    }
    if (!LAST) {
#pragma unroll
        for (int rd_ = 0; rd_ < 3; ++rd_) {
            int it = (rd_ * 4 + w) * 64 + lane;
            if (it >= 640) it = lane;
            const int row = 80 * j + (it >> 3), ch = it & 7;
            resid[rd_] = lds_rd128(L, row * 128 + (((ch >> 1) ^ ((row >> 1) & 3)) * 32) + (ch & 1) * 16);
        }
    }
    __syncthreads();
    float rstd[4];
#pragma unroll
    for (int u = 0; u < 4; ++u)
        rstd[u] = __builtin_amdgcn_rsqf(((LAS float *)(L + L_SS))[16 * ((u + w) & 3) + n] * (1.0f / 640.0f) + 1e-5f);
    const unsigned hoff = OFF_H + (LAST ? 1 : 0) * H_BYTES + i * (48 * 4096);
    v2u hp[3][4];
#pragma unroll
    for (int r = 0; r < 3; ++r)
#pragma unroll
        for (int u = 0; u < 4; ++u) {
            float y[4];
#pragma unroll
            for (int e = 0; e < 4; ++e) y[e] = gelu_f(acc[r][u][e] * rstd[u] + b1v[4 * r + e]);
            hp[r][u] = v2u{pk_bf(y[0], y[1]), pk_bf(y[2], y[3])};
        }
    const int ft0 = 12 * j + 3 * w;
    if ((w & 1) == 0) {
#pragma unroll
        for (int u = 0; u < 4; ++u) {
            const int tt = (u + w) & 3;
            st_sc1(v4u{hp[0][u].x, hp[0][u].y, hp[1][u].x, hp[1][u].y}, rs, lane * 16, hoff + (ft0 >> 1) * 4096 + tt * 1024);
            __builtin_amdgcn_raw_buffer_store_b64(hp[2][u], rs, lane * 16, hoff + ((ft0 + 2) >> 1) * 4096 + tt * 1024, AUX_SC1);
        }
    } else {
#pragma unroll
        for (int u = 0; u < 4; ++u) {
            const int tt = (u + w) & 3;
            __builtin_amdgcn_raw_buffer_store_b64(hp[0][u], rs, lane * 16 + 8, hoff + (ft0 >> 1) * 4096 + tt * 1024, AUX_SC1);
            st_sc1(v4u{hp[1][u].x, hp[1][u].y, hp[2][u].x, hp[2][u].y}, rs, lane * 16, hoff + ((ft0 + 1) >> 1) * 4096 + tt * 1024);
        }
    }
}

__device__ __forceinline__ void phase_r(LAS unsigned char *L, __amdgpu_buffer_rsrc_t rs, int j, int w, int lane, f32x4 (&acc)[5][4]) {
    const int g = lane >> 4;
    int tra[4];
    {
        const int q4 = (lane >> 2) & 3, pi = lane & 3, row = 4 * g + q4;
#pragma unroll
        for (int u = 0; u < 4; ++u) tra[u] = (5 * w) * 4096 + row * 128 + ((u ^ ((row >> 1) & 3)) * 32) + pi * 8;
    }
    v4u a[25];
#pragma unroll
    for (int f = 0; f < 25; ++f) a[f] = ld_pl(rs, lane * 16, OFF_RB2 + ((j * 4 + w) * 25 + f) * 1024);
#pragma unroll
    for (int ks = 0; ks < 5; ++ks) {
        v4u b[4];
#pragma unroll
        for (int u = 0; u < 4; ++u) {
            const v2u lo = lds_tr(L, tra[u] + ks * 4096), hi = lds_tr(L, tra[u] + ks * 4096 + 2048);
            b[u] = v4u{lo.x, lo.y, hi.x, hi.y};
        }
#pragma unroll
        for (int m = 0; m < 5; ++m)
#pragma unroll
            for (int u = 0; u < 4; ++u) {
                if (ks == 0) mf_vv0(acc[m][u], a[ks * 5 + m], b[u]);
                else mf_vv(acc[m][u], a[ks * 5 + m], b[u]);
            }
    }
}

template <bool LAST>
__device__ __forceinline__ void phase_g2(const Params &p, LAS unsigned char *L, __amdgpu_buffer_rsrc_t rs, int i, int j, int w, int lane,
                                         f32x4 (&acc)[5][4], const v4u (&resid)[3]) {
    const int g = lane >> 4, n = lane & 15;
    const unsigned hoff = OFF_H + (LAST ? 1 : 0) * H_BYTES + i * (48 * 4096) + (12 * w) * 4096;
    v4u h[3][4];
#pragma unroll
    for (int u = 0; u < 4; ++u) {
        h[0][u] = ld_sc1(rs, lane * 16, hoff + u * 1024);
        h[1][u] = ld_sc1(rs, lane * 16, hoff + 4096 + u * 1024);
    }
    static_for<0, 12>([&](auto kc) {
        constexpr int ks = kc.value;
        if constexpr (ks + 2 < 12) {
#pragma unroll
            for (int u = 0; u < 4; ++u) h[(ks + 2) % 3][u] = ld_sc1(rs, lane * 16, hoff + (ks + 2) * 4096 + u * 1024);
        }
        static_for<0, 5>([&](auto mc) {
            constexpr int m = mc.value;
            static_for<0, 4>([&](auto uc) {
                constexpr int u = uc.value;
                if constexpr (LAST) mf_ra<ks * 5 + m>(acc[m][u], h[ks % 3][u]);
                else if constexpr (ks == 0) mf_rb0<ks * 5 + m>(acc[m][u], h[ks % 3][u]);
                else mf_rb<ks * 5 + m>(acc[m][u], h[ks % 3][u]);
            });
        });
        __builtin_amdgcn_sched_barrier(0);
    });
    asm volatile("s_nop 7" : "+v"(acc[0][0]), "+v"(acc[0][1]), "+v"(acc[0][2]), "+v"(acc[0][3]), "+v"(acc[1][0]), "+v"(acc[1][1]), "+v"(acc[1][2]),
                 "+v"(acc[1][3]), "+v"(acc[2][0]), "+v"(acc[2][1]), "+v"(acc[2][2]), "+v"(acc[2][3]));
    asm volatile("s_nop 7" : "+v"(acc[3][0]), "+v"(acc[3][1]), "+v"(acc[3][2]), "+v"(acc[3][3]), "+v"(acc[4][0]), "+v"(acc[4][1]), "+v"(acc[4][2]),
                 "+v"(acc[4][3]));
    TS(LAST ? 23 : 10);
    if (!LAST) {
#pragma unroll
        for (int m = 0; m < 5; ++m)
#pragma unroll
            for (int u = 0; u < 4; ++u) *(LAS f32x4 *)(L + w * L_PART + (16 * m + n) * 272 + (16 * u + 4 * g) * 4) = acc[m][u];
        __syncthreads();
        const unsigned xout = OFF_XH + 2 * XH_BYTES;
        float ssq[8];
#pragma unroll
        for (int e = 0; e < 8; ++e) ssq[e] = 0.f;
#pragma unroll
        for (int rd_ = 0; rd_ < 3; ++rd_) {
            const int it = (rd_ * 4 + w) * 64 + lane;
            if (rd_ < 2 || w < 2) {
                const int row = it >> 3, ch = it & 7;
                f32x4 s0 = {0.f, 0.f, 0.f, 0.f}, s1 = {0.f, 0.f, 0.f, 0.f};
#pragma unroll
                for (int ww = 0; ww < 4; ++ww) {
                    s0 += *(LAS f32x4 *)(L + ww * L_PART + row * 272 + ch * 32);
                    s1 += *(LAS f32x4 *)(L + ww * L_PART + row * 272 + ch * 32 + 16);
                }
                const int kp = 80 * j + row;
                const float bb = *(const GAS float *)((const GAS unsigned char *)p.ws + OFF_B2P + kp * 4);
                float v[8];
#pragma unroll
                for (int e = 0; e < 8; ++e) {
                    const unsigned u = resid[rd_][e >> 1];
                    v[e] = ((e & 1) ? bf_hi(u) : bf_lo(u)) + (e < 4 ? s0[e & 3] : s1[e & 3]) + bb;
                }
                const v4u o = {pk_bf(v[0], v[1]), pk_bf(v[2], v[3]), pk_bf(v[4], v[5]), pk_bf(v[6], v[7])};
                st_sc1(o, rs, kp * 4096 + i * 128 + ch * 16, xout);
                if (kp != 1) {
#pragma unroll
                    for (int e = 0; e < 8; ++e) {
                        const float z = (e & 1) ? bf_hi(o[e >> 1]) : bf_lo(o[e >> 1]);
                        ssq[e] += z * z;
                    }
                }
            }
        }
#pragma unroll
        for (int e = 0; e < 8; ++e) {
            ssq[e] += __shfl_xor(ssq[e], 8);
            ssq[e] += __shfl_xor(ssq[e], 16);
            ssq[e] += __shfl_xor(ssq[e], 32);
        }
        if (lane < 8) {
            *(LAS f32x4 *)(L + L_SSP + w * 256 + lane * 32) = f32x4{ssq[0], ssq[1], ssq[2], ssq[3]};
            *(LAS f32x4 *)(L + L_SSP + w * 256 + lane * 32 + 16) = f32x4{ssq[4], ssq[5], ssq[6], ssq[7]};
        }
        __syncthreads();
        if (w == 0) {
            const LAS float *sp = (const LAS float *)(L + L_SSP);
            const float t = (sp[lane] + sp[64 + lane]) + (sp[128 + lane] + sp[192 + lane]);
            __hip_atomic_fetch_add((GAS double *)((GAS unsigned char *)p.ws + OFF_SS + 16384) + 64 * i + lane, (double)t, RLX_AGENT);
        }
    } else {
#pragma unroll
        for (int m = 0; m < 5; ++m)
#pragma unroll
            for (int u = 0; u < 4; ++u) *(LAS f32x4 *)(L + w * L_PART + (16 * u + n) * 336 + (16 * m + 4 * g) * 4) = acc[m][u];
        __syncthreads();
#pragma unroll
        for (int rd_ = 0; rd_ < 3; ++rd_) {
            const int it = (rd_ * 4 + w) * 64 + lane;
            if (rd_ < 2 || w < 2) {
                const int tok = it / 10, ch = it - tok * 10;
                f32x4 s0 = {0.f, 0.f, 0.f, 0.f}, s1 = {0.f, 0.f, 0.f, 0.f};
#pragma unroll
                for (int ww = 0; ww < 4; ++ww) {
                    s0 += *(LAS f32x4 *)(L + ww * L_PART + tok * 336 + ch * 32);
                    s1 += *(LAS f32x4 *)(L + ww * L_PART + tok * 336 + ch * 32 + 16);
                }
                const int d0 = 80 * j + 8 * ch;
                const v4u bb = *(const GAS v4u *)((const GAS unsigned char *)p.b2 + d0 * 2);
                float v[8];
#pragma unroll
                for (int e = 0; e < 8; ++e) v[e] = (e < 4 ? s0[e & 3] : s1[e & 3]) + ((e & 1) ? bf_hi(bb[e >> 1]) : bf_lo(bb[e >> 1]));
                const v4u o = {pk_bf(v[0], v[1]), pk_bf(v[2], v[3]), pk_bf(v[4], v[5]), pk_bf(v[6], v[7])};
                *(GAS v4u *)((GAS unsigned char *)p.out + pos2tok(64 * i + tok) * 1280 + d0 * 2) = o;
            }
        }
    }
}

template <int PB, int PE> __global__ void __launch_bounds__(256, 1) k_main(Params p) {
    extern __shared__ __attribute__((aligned(16))) unsigned char smem[];
    LAS unsigned char *L = (LAS unsigned char *)smem;
    asm volatile("" ::: "a239");
    const __amdgpu_buffer_rsrc_t rs = __builtin_amdgcn_make_buffer_rsrc((void *)p.ws, 0, (int)WS_USED, RSRC_W3);
    const int lane = threadIdx.x & 63, w = __builtin_amdgcn_readfirstlane(threadIdx.x >> 6);
    const int bid = blockIdx.x, slot = bid >> 3;
    const int i = 4 * (bid & 7) + (slot >> 3), j = slot & 7;
    gu32 *ctl = (gu32 *)(p.ws + OFF_CTRL);
    const GAS unsigned char *wsg = (const GAS unsigned char *)p.ws;
    const GAS unsigned char *run1 = wsg + OFF_W1F + (j * 4 + w) * 61440;
    const GAS unsigned char *run2 = wsg + OFF_W2F + (j * 4 + w) * 61440;
    const GAS unsigned char *run3 = wsg + OFF_W2T + (j * 4 + w) * 61440;
    const unsigned voffr = lane * 16;
    const bool isC = bid < 160;
    TS(0);

    if constexpr (PB == 0) {
        if (isC) {
            CConst K;
            c_consts(K, w, lane);
            TS(1);
            phase_c<false>(K, L, rs, bid, 0, w, lane);
            TS(2);
        }
        if constexpr (PE > 1) {
            if (isC) signal_add(ctl + CW_BAR + (0 * 16 + (bid & 15)) * 64);
            TS(3);
        }
    }
    if constexpr (PB <= 1 && PE > 1) {
        ring_fill<0, 60>(run1, voffr);
        if constexpr (PB == 0) {
            if (!wait_words<16>(ctl + CW_BAR + 0 * 16 * 64, 10u, ctl + CW_TMO, L)) return;
        }
        TS(4);
        v4u resid[3];
        phase_g1<false>(p, L, rs, i, j, w, lane, run2, resid);
        TS(7);
        signal_add(ctl + CW_GRP + i * 64);
        TS(8);
        if (!wait_words<1>(ctl + CW_GRP + i * 64, 8u, ctl + CW_TMO, L)) return;
        TS(9);
        f32x4 acc[5][4];
        phase_g2<false>(p, L, rs, i, j, w, lane, acc, resid);
        TS(11);
        if constexpr (PE > 2) signal_add(ctl + CW_BAR + (1 * 16 + (bid & 15)) * 64);
        TS(12);
    }
    if constexpr (PB <= 2 && PE > 2) {
        if (isC) {
            CConst K;
            c_consts(K, w, lane);
            if constexpr (PB < 2) {
                if (!wait_words<16>(ctl + CW_BAR + 1 * 16 * 64, 16u, ctl + CW_TMO, L)) return;
            }
            TS(13);
            phase_c<true>(K, L, rs, bid, 1, w, lane);
            TS(14);
            if constexpr (PE > 3) signal_add(ctl + CW_BAR + (2 * 16 + (bid & 15)) * 64);
            TS(15);
        }
    }
    if constexpr (PE > 3) {
        ring_fill<0, 60>(run1, voffr);
        if constexpr (PB < 3) {
            if (!wait_words<16>(ctl + CW_BAR + 2 * 16 * 64, 10u, ctl + CW_TMO, L)) return;
        }
        TS(16);
        v4u resid[3];
        phase_g1<true>(p, L, rs, i, j, w, lane, run3, resid);
        TS(19);
        signal_add(ctl + CW_GRP + (32 + i) * 64);
        TS(20);
        f32x4 acc[5][4];
        phase_r(L, rs, j, w, lane, acc);
        TS(21);
        if (!wait_words<1>(ctl + CW_GRP + (32 + i) * 64, 8u, ctl + CW_TMO, L)) return;
        TS(22);
        phase_g2<true>(p, L, rs, i, j, w, lane, acc, resid);
        TS(24);
    }
    TS_DUMP();
}

__device__ __forceinline__ int col_bin(int c) {
    const int k1 = c >> 5, k2 = c & 31;
    const int a = k2 < 16 ? k1 + 20 * k2 : 640 - k1 - 20 * k2;
    return c < 16 ? 20 * c : c < 32 ? 10 + 20 * (c - 16) : a;
}
__device__ __forceinline__ float kap_bin(int kap) { return kap == 0 ? 320.0f : (float)col_bin(kap >> 1); }
__device__ __forceinline__ float kap_phase(int kap) { return (kap > 1 && (kap & 1)) ? 0.25f : 0.0f; }

constexpr int A_T = 0;
constexpr int A_Y = 41216;
constexpr int A_RED = A_Y + 41600;
constexpr int A_BYTES = 84 * 1024;
constexpr int N_X = 64, N_W1 = 48, N_W2 = 48, N_RB = 20, N_WT = 40, N_W2T = 24;
constexpr int B_W1 = N_X, B_W2 = B_W1 + N_W1, B_B2 = B_W2 + N_W2, B_RB = B_B2 + 1, B_WT = B_RB + N_RB, B_W2T = B_WT + N_WT, A_GRID = B_W2T + N_W2T;

__device__ __forceinline__ f32x4 mfma16(v4u a, v4u b, f32x4 c) {
    return __builtin_amdgcn_mfma_f32_16x16x32_bf16(__builtin_bit_cast(bf16x8, a), __builtin_bit_cast(bf16x8, b), c, 0, 0, 0);
}
#define PREP_AUX AUX_SC1
__device__ __forceinline__ void st_ws(v4u v, __amdgpu_buffer_rsrc_t rs, int voff, int soff) {
    __builtin_amdgcn_raw_buffer_store_b128(v, rs, voff, soff, PREP_AUX);
}

template <int KIND>
__device__ __forceinline__ void dft_task(const Params &p, LAS unsigned char *L, __amdgpu_buffer_rsrc_t rs, int task, int w, int lane) {
    const int g = lane >> 4, n = lane & 15;
    v4u ld[KIND == 1 ? 10 : 12];
    if constexpr (KIND == 1) {
#pragma unroll
        for (int i = 0; i < 10; ++i) {
            const int rp = 16 * w + 64 * i + (lane >> 2), d2 = (rp * 3277) >> 16, d1 = rp - 20 * d2;
            ld[i] = *(const GAS v4u *)((const GAS unsigned char *)p.w1 + (32 * d1 + d2) * 3072 + task * 64 + (lane & 3) * 16);
        }
    } else {
        const int rq = lane & 7, cc = lane >> 3;
        const GAS unsigned char *rowp[4];
#pragma unroll
        for (int e = 0; e < 4; ++e) {
            if constexpr (KIND == 0) rowp[e] = (const GAS unsigned char *)p.x + pos2tok(32 * task + 8 * (rq & 3) + 4 * (rq >> 2) + e) * 1280;
            else if constexpr (KIND == 2) rowp[e] = (const GAS unsigned char *)p.w2 + (32 * task + 4 * rq + e) * 1280;
            else rowp[e] = (const GAS unsigned char *)p.b2;
        }
#pragma unroll
        for (int i = 0; i < 3; ++i) {
            const int cg = (i < 2 || w < 2) ? w + 4 * i : w;
#pragma unroll
            for (int e = 0; e < 4; ++e) ld[4 * i + e] = *(const GAS v4u *)(rowp[e] + (8 * cg + cc) * 16);
        }
    }
    v4u f20[2];
#pragma unroll
    for (int u = 0; u < 2; ++u) {
        const int s = 16 * u + n;
        const float k1 = (float)(s == 0 ? 0 : s == 1 ? 10 : s >> 1), ph = (s > 1 && (s & 1)) ? 0.25f : 0.0f;
        float c[8];
#pragma unroll
        for (int j = 0; j < 8; ++j) {
            const int d1 = j < 4 ? 4 * g + j : 12 + 4 * g + j;
            const float v = __builtin_amdgcn_cosf((float)d1 * k1 * 0.05f + ph) * 0.055901699f;
            c[j] = (s < 20 && d1 < 20) ? v : 0.0f;
        }
        f20[u] = v4u{pk_bf(c[0], c[1]), pk_bf(c[2], c[3]), pk_bf(c[4], c[5]), pk_bf(c[6], c[7])};
    }
    v4u b1[5][2], b2[5][2];
#pragma unroll
    for (int b = 0; b < 5; ++b)
#pragma unroll
        for (int u = 0; u < 2; ++u) {
            const int ks = 5 * w + b, kap = 32 * ks + 16 * u + n;
            const float kb = kap_bin(kap) * (1.0f / 640.0f), ph = kap_phase(kap);
            float c[8];
#pragma unroll
            for (int j = 0; j < 8; ++j) c[j] = __builtin_amdgcn_cosf((float)(16 * (j >> 2) + 4 * g + (j & 3)) * kb + ph);
            if (b == 0 && u == 0) {
                const float sc = kap == 1 && KIND == 1 ? 0.0f : kap < 2 ? 0.70710678f : 1.0f;
#pragma unroll
                for (int j = 0; j < 8; ++j) c[j] *= sc;
            }
            const unsigned sg = ks < 2 ? 0u : (((ks & 1) ^ (n & 1) ^ 1) ? 0x80008000u : 0u);
#pragma unroll
            for (int h = 0; h < 4; ++h) {
                b1[b][u][h] = pk_bf(c[2 * h], c[2 * h + 1]);
                const unsigned o = (unsigned)__builtin_amdgcn_update_dpp(0, (int)b1[b][u][h], 0xB1, 0xF, 0xF, true);
                b2[b][u][h] = ks < 2 ? 0u : o ^ sg;
            }
        }
    if constexpr (KIND == 1) {
#pragma unroll
        for (int i = 0; i < 10; ++i) {
            const int rp = 16 * w + 64 * i + (lane >> 2), d2 = (rp * 3277) >> 16, rho = rp + (d2 >> 3), ch = lane & 3;
            *(LAS v4u *)(L + A_T + rho * 64 + (((ch >> 1) ^ ((rho >> 2) & 1)) * 32) + (ch & 1) * 16) = ld[i];
        }
    } else {
        const int rq = lane & 7, cc = lane >> 3;
#pragma unroll
        for (int i = 0; i < 3; ++i) {
            if (i < 2 || w < 2) {
                const int c = 8 * (w + 4 * i) + cc, rho0 = 161 * (c & 3) + (c >> 2);
                const int a0 = A_T + rho0 * 64 + (((rq >> 2) ^ ((rho0 >> 2) & 1)) * 32) + (rq & 3) * 8, a1 = a0 ^ 32;
#pragma unroll
                for (int e = 0; e < 8; ++e) {
                    const unsigned sel = (e & 1) ? 0x07060302u : 0x05040100u;
                    const v2u o = {__builtin_amdgcn_perm(ld[4 * i + 1][e >> 1], ld[4 * i][e >> 1], sel),
                                   __builtin_amdgcn_perm(ld[4 * i + 3][e >> 1], ld[4 * i + 2][e >> 1], sel)};
                    *(LAS v2u *)(L + ((e & 1) ? a1 : a0) + e * 1280) = o;
                }
            }
        }
    }
    __syncthreads();
    const int q4 = (lane >> 2) & 3, pi = lane & 3;
    {
        const int rl = 161 * w + 4 * g + q4, rh = 161 * w + 16 + q4;
        const int al = A_T + rl * 64 + ((rl >> 2) & 1) * 32 + pi * 8, ah = A_T + rh * 64 + ((rh >> 2) & 1) * 32 + pi * 8;
        const int yw = A_Y + n * 2080 + 8 * w * 64 + g * 8;
        v2u hi4[8][2];
#pragma unroll
        for (int e = 0; e < 8; ++e)
#pragma unroll
            for (int t = 0; t < 2; ++t) {
                const int x = ((e + t) & 1) * 32;
                const v2u lo = lds_tr(L, (al ^ x) + e * 1280), hi = lds_tr(L, (ah ^ x) + e * 1280);
                const v4u a = {lo.x, lo.y, hi.x, hi.y};
                const f32x4 z = {0.f, 0.f, 0.f, 0.f};
                const f32x4 y0 = mfma16(a, f20[0], z), y1 = mfma16(a, f20[1], z);
                *(LAS v2u *)(L + yw + e * 64 + ((t ^ ((e >> 2) & 1)) * 32)) = v2u{pk_bf(y0[0], y0[1]), pk_bf(y0[2], y0[3])};
                hi4[e][t] = v2u{pk_bf(y1[0], y1[1]), pk_bf(y1[2], y1[3])};
            }
        if (n < 4) {
#pragma unroll
            for (int e = 0; e < 8; ++e)
#pragma unroll
                for (int t = 0; t < 2; ++t) *(LAS v2u *)(L + yw + 16 * 2080 + e * 64 + ((t ^ ((e >> 2) & 1)) * 32)) = hi4[e][t];
        }
    }
    __syncthreads();
    float ssq[8];
#pragma unroll
    for (int e = 0; e < 8; ++e) ssq[e] = 0.f;
    const int yr0 = A_Y + (4 * g + q4) * 64 + (g & 1) * 32 + pi * 8;
#pragma unroll
    for (int b = 0; b < 5; ++b) {
        const int ks = 5 * w + b, sr = ks < 2 ? ks : ks & ~1, si = ks | 1;
        v4u yr[2], yi[2];
#pragma unroll
        for (int t = 0; t < 2; ++t) {
            const int ar = (yr0 ^ (t * 32)) + sr * 2080, ai = (yr0 ^ (t * 32)) + si * 2080;
            const v2u rl = lds_tr(L, ar), rh = lds_tr(L, ar + 1024), il = lds_tr(L, ai), ih = lds_tr(L, ai + 1024);
            yr[t] = v4u{rl.x, rl.y, rh.x, rh.y};
            yi[t] = v4u{il.x, il.y, ih.x, ih.y};
        }
        f32x4 o[2][2];
        const f32x4 z = {0.f, 0.f, 0.f, 0.f};
#pragma unroll
        for (int u = 0; u < 2; ++u)
#pragma unroll
            for (int t = 0; t < 2; ++t) {
                if constexpr (KIND == 1) o[u][t] = mfma16(b2[b][u], yi[t], mfma16(b1[b][u], yr[t], z));
                else o[u][t] = mfma16(yi[t], b2[b][u], mfma16(yr[t], b1[b][u], z));
            }
        if constexpr (KIND == 1) {
#pragma unroll
            for (int t = 0; t < 2; ++t) {
                const int ft = 2 * task + t, j = ft / 12, r3 = ft - 12 * j, ww = r3 / 3, r = r3 - 3 * ww;
                const v4u v = {pk_bf(o[0][t][0], o[0][t][1]), pk_bf(o[0][t][2], o[0][t][3]), pk_bf(o[1][t][0], o[1][t][1]), pk_bf(o[1][t][2], o[1][t][3])};
                st_ws(v, rs, lane * 16, OFF_W1F + ((j * 4 + ww) * 60 + ks * 3 + r) * 1024);
            }
        } else {
#pragma unroll
            for (int u = 0; u < 2; ++u) {
                const int tau = 2 * ks + u;
                const v4u v = {pk_bf(o[u][0][0], o[u][0][1]), pk_bf(o[u][0][2], o[u][0][3]), pk_bf(o[u][1][0], o[u][1][1]), pk_bf(o[u][1][2], o[u][1][3])};
                if constexpr (KIND == 0) {
                    st_ws(v, rs, n * 4096 + g * 16, OFF_XH + tau * 16 * 4096 + task * 64);
                    const bool dc = (b == 0 && u == 0) && w == 0 && n == 1;
#pragma unroll
                    for (int e = 0; e < 8; ++e) {
                        const float z1 = dc ? 0.f : o[u][e >> 2][e & 3];
                        ssq[e] += z1 * z1;
                    }
                } else if constexpr (KIND == 2) {
                    const int j = tau / 5, m = tau - 5 * j, ww = task / 12, kf = task - 12 * ww;
                    st_ws(v, rs, lane * 16, OFF_W2F + ((j * 4 + ww) * 60 + kf * 5 + m) * 1024);
                } else {
                    if (g == 0) ((GAS float *)(p.ws + OFF_B2P))[16 * tau + n] = o[u][0][0];
                }
            }
        }
    }
    if constexpr (KIND == 0) {
#pragma unroll
        for (int e = 0; e < 8; ++e) {
            float v = ssq[e];
            v += __builtin_bit_cast(float, __builtin_amdgcn_update_dpp(0, __builtin_bit_cast(int, v), 0xB1, 0xF, 0xF, true));
            v += __builtin_bit_cast(float, __builtin_amdgcn_update_dpp(0, __builtin_bit_cast(int, v), 0x4E, 0xF, 0xF, true));
            v += __builtin_bit_cast(float, __builtin_amdgcn_update_dpp(0, __builtin_bit_cast(int, v), 0x141, 0xF, 0xF, true));
            v += __builtin_bit_cast(float, __builtin_amdgcn_update_dpp(0, __builtin_bit_cast(int, v), 0x140, 0xF, 0xF, true));
            ssq[e] = v;
        }
        LAS float *red = (LAS float *)(L + A_RED);
        if (n == 0) {
            *(LAS f32x4 *)(red + 32 * w + 8 * g) = f32x4{ssq[0], ssq[1], ssq[2], ssq[3]};
            *(LAS f32x4 *)(red + 32 * w + 8 * g + 4) = f32x4{ssq[4], ssq[5], ssq[6], ssq[7]};
        }
        __syncthreads();
        if (threadIdx.x < 32) {
            const int t = threadIdx.x;
            const float s = (red[t] + red[32 + t]) + (red[64 + t] + red[96 + t]);
            ((GAS double *)(p.ws + OFF_SS))[32 * task + t] = (double)s;
        }
    }
}

__device__ __forceinline__ void rb2_task(__amdgpu_buffer_rsrc_t rs, int ks, int w, int lane) {
    const int g = lane >> 4, n = lane & 15;
    float kb[8], ph[8];
#pragma unroll
    for (int jj = 0; jj < 8; ++jj) {
        const int kap = 32 * ks + 16 * (jj >> 2) + 4 * g + (jj & 3);
        kb[jj] = kap_bin(kap);
        ph[jj] = kap_phase(kap);
    }
    const float sc01 = (ks == 0 && g == 0) ? 0.039528471f : 0.055901699f;
#pragma unroll
    for (int jh = 0; jh < 2; ++jh)
#pragma unroll
        for (int m = 0; m < 5; ++m) {
            const int j = 2 * w + jh;
            const float d = (float)(80 * j + 16 * m + n);
            float c[8];
#pragma unroll
            for (int jj = 0; jj < 8; ++jj)
                c[jj] = __builtin_amdgcn_cosf(__builtin_amdgcn_fractf(kb[jj] * d * (1.0f / 640.0f)) + ph[jj]) * (jj < 2 ? sc01 : 0.055901699f);
            const v4u v = {pk_bf(c[0], c[1]), pk_bf(c[2], c[3]), pk_bf(c[4], c[5]), pk_bf(c[6], c[7])};
            st_ws(v, rs, lane * 16, OFF_RB2 + ((j * 4 + ks / 5) * 25 + (ks % 5) * 5 + m) * 1024);
        }
}

__device__ __forceinline__ int bin_col(int k) {
    const int q = k / 20, r = k - 20 * q;
    return k == 320 ? 0 : r == 0 ? q : r == 10 ? 16 + q : r < 10 ? 32 * r + q : 32 * (20 - r) + 31 - q;
}
typedef v4u v4u_a2 __attribute__((aligned(2)));
__device__ __forceinline__ void wt_task(const Params &p, __amdgpu_buffer_rsrc_t rs, int task, int ww, int lane) {
    const int w = task & 3, part = (task >> 2) & 1, range = task >> 3;
    const int g = lane >> 4, cl = lane & 15;
    const GAS uint16_t *src = (const GAS uint16_t *)(part ? p.wi : p.wr);
    const int col0 = 1 + 64 * range + 16 * ww;
    v4u v[2][8];
#pragma unroll
    for (int e = 0; e < 8; ++e) {
        const int jf = 16 * w + cl + 64 * (16 * (e >> 2) + 4 * g + (e & 3));
#pragma unroll
        for (int h = 0; h < 2; ++h) v[h][e] = *(const GAS v4u_a2 *)(src + jf * 321 + col0 + 8 * h);
    }
#pragma unroll
    for (int h = 0; h < 2; ++h)
#pragma unroll
        for (int cc = 0; cc < 8; ++cc) {
            const unsigned sel = (cc & 1) ? 0x07060302u : 0x05040100u;
            v4u o;
#pragma unroll
            for (int d = 0; d < 4; ++d) o[d] = __builtin_amdgcn_perm(v[h][2 * d + 1][cc >> 1], v[h][2 * d][cc >> 1], sel);
            const int c = bin_col(col0 + 8 * h + cc);
            st_ws(o, rs, lane * 16, OFF_WT + (2 * c + part) * 4096 + w * 1024);
        }
}

__device__ __forceinline__ void w2t_task(const Params &p, LAS unsigned char *L, __amdgpu_buffer_rsrc_t rs, int task, int w, int lane) {
    v4u t[20];
    const GAS unsigned char *src = (const GAS unsigned char *)p.w2 + (64 * task + 16 * w) * 1280;
#pragma unroll
    for (int i = 0; i < 20; ++i) t[i] = *(const GAS v4u *)(src + (64 * i + lane) * 16);
#pragma unroll
    for (int i = 0; i < 20; ++i) {
        const int idx = 64 * i + lane, row = (idx * 52429) >> 22, ch = idx - 80 * row;
        *(LAS v4u *)(L + (16 * w + row) * 1312 + ch * 16) = t[i];
    }
    __syncthreads();
    const int g = lane >> 4, q4 = (lane >> 2) & 3, pi = lane & 3;
    const int a0 = (4 * g + q4) * 1312 + pi * 8;
#pragma unroll
    for (int fg = 0; fg < 2; ++fg)
#pragma unroll
        for (int i = 0; i < 10; ++i) {
            const int tau = 10 * w + i, j = tau / 5, m = tau - 5 * j;
            const int grp = 2 * task + fg, ww = grp / 12, ks = grp - 12 * ww;
            const v2u lo = lds_tr(L, a0 + fg * 32 * 1312 + tau * 32), hi = lds_tr(L, a0 + (fg * 32 + 16) * 1312 + tau * 32);
            st_ws(v4u{lo.x, lo.y, hi.x, hi.y}, rs, lane * 16, OFF_W2T + ((j * 4 + ww) * 60 + ks * 5 + m) * 1024);
        }
}

__global__ void __launch_bounds__(256, 2) k_prep(Params p) {
    extern __shared__ __attribute__((aligned(16))) unsigned char smem[];
    LAS unsigned char *L = (LAS unsigned char *)smem;
    const __amdgpu_buffer_rsrc_t rs = __builtin_amdgcn_make_buffer_rsrc((void *)p.ws, 0, (int)WS_USED, RSRC_W3);
    const int lane = threadIdx.x & 63, w = __builtin_amdgcn_readfirstlane(threadIdx.x >> 6);
    const int b = blockIdx.x;
    if (b < B_W1) dft_task<0>(p, L, rs, b, w, lane);
    else if (b < B_W2) dft_task<1>(p, L, rs, b - B_W1, w, lane);
    else if (b < B_B2) dft_task<2>(p, L, rs, b - B_W2, w, lane);
    else if (b < B_RB) dft_task<3>(p, L, rs, 0, w, lane);
    else if (b < B_WT) {
        const int k = b - B_RB;
        if (k < 16) st_ws(v4u{0u, 0u, 0u, 0u}, rs, threadIdx.x * 16, OFF_CTRL + k * 4096);
        else st_ws(v4u{0u, 0u, 0u, 0u}, rs, threadIdx.x * 16, OFF_SS + 16384 + (k - 16) * 4096);
        rb2_task(rs, k, w, lane);
    } else if (b < B_W2T) wt_task(p, rs, b - B_WT, w, lane);
    else w2t_task(p, L, rs, b - B_W2T, w, lane);
}

static void fill_params(Params &p, void *const *d_in, void *d_out, void *d_ws) {
    p.x = (const uint16_t *)d_in[0];
    p.wr = (const uint16_t *)d_in[1];
    p.wi = (const uint16_t *)d_in[2];
    p.w1 = (const uint16_t *)d_in[4];
    p.b1 = (const uint16_t *)d_in[5];
    p.w2 = (const uint16_t *)d_in[6];
    p.b2 = (const uint16_t *)d_in[7];
    p.out = (uint16_t *)d_out;
    p.ws = (unsigned char *)d_ws;
}
static void launch_prep(const Params &p, hipStream_t stream) {
    hipFuncSetAttribute((const void *)k_prep, hipFuncAttributeMaxDynamicSharedMemorySize, A_BYTES);
    k_prep<<<A_GRID, 256, A_BYTES, stream>>>(p);
}
template <int PB, int PE> static void launch_main(const Params &p, hipStream_t stream) {
    hipFuncSetAttribute((const void *)k_main<PB, PE>, hipFuncAttributeMaxDynamicSharedMemorySize, L_BYTES);
    k_main<PB, PE><<<256, 256, L_BYTES, stream>>>(p);
}

extern "C" void kernel_launch(void *const *d_in, const int *in_sizes, int n_in, void *d_out, int out_size, void *d_ws, size_t ws_size,
                              hipStream_t stream) {
    Params p;
    fill_params(p, d_in, d_out, d_ws);
    launch_prep(p, stream);
    launch_main<0, 4>(p, stream);
}
```

```cpp
#include <hip/hip_runtime.h>
#include <stdint.h>
#include <type_traits>

#define GAS __attribute__((address_space(1)))
#define LAS __attribute__((address_space(3)))
typedef unsigned v4u __attribute__((ext_vector_type(4)));
typedef unsigned v2u __attribute__((ext_vector_type(2)));
typedef float f32x4 __attribute__((ext_vector_type(4)));
typedef float f32x2 __attribute__((ext_vector_type(2)));
typedef short s16x4 __attribute__((ext_vector_type(4)));
typedef __bf16 bf16x2 __attribute__((ext_vector_type(2)));
typedef __bf16 bf16x8 __attribute__((ext_vector_type(8)));
typedef GAS unsigned gu32;
#define RLX_AGENT __ATOMIC_RELAXED, __HIP_MEMORY_SCOPE_AGENT

constexpr int S_ = 2048, D_ = 640, F_ = 1536;
constexpr int AUX_SC1 = 16;
constexpr int RSRC_W3 = 0x00020000;

constexpr unsigned OFF_CTRL = 0;
constexpr unsigned OFF_SS = 65536;
constexpr unsigned OFF_B2P = OFF_SS + 32768;
constexpr unsigned XH_BYTES = D_ * S_ * 2;
constexpr unsigned OFF_XH = 131072;
constexpr unsigned H_BYTES = S_ * F_ * 2;
constexpr unsigned OFF_H = OFF_XH + 4 * XH_BYTES;
constexpr unsigned WF_BYTES = D_ * F_ * 2;
constexpr unsigned OFF_W1F = OFF_H + 2 * H_BYTES;
constexpr unsigned OFF_W2F = OFF_W1F + WF_BYTES;
constexpr unsigned OFF_W2T = OFF_W2F + WF_BYTES;
constexpr unsigned OFF_RB2 = OFF_W2T + WF_BYTES;
constexpr unsigned OFF_WT = OFF_RB2 + D_ * D_ * 2;
constexpr unsigned WS_USED = OFF_WT + 320 * 8192;

constexpr int CW_TMO = 0, CW_BAR = 1024, CW_GRP = 8192;
constexpr unsigned SPIN_CAP = 1u << 16;

constexpr int L_TILE = 0;
constexpr int L_PART = 21760;
constexpr int L_MISC = 4 * L_PART;
constexpr int L_SS = L_MISC;
constexpr int L_SSP = L_MISC + 256;
constexpr int L_FLAG = L_MISC + 256 + 1024;
constexpr int L_TS = L_MISC + 2048;
constexpr int L_BYTES = L_MISC + 2048 + 256;
constexpr unsigned OFF_TS = 200u << 20;
#define TS(n)
#define TS_DUMP()

template <int I, int N, class Fn> __device__ __forceinline__ void static_for(Fn &&f) {
    if constexpr (I < N) {
        f(std::integral_constant<int, I>{});
        static_for<I + 1, N>(f);
    }
}

__device__ __forceinline__ float bf_lo(unsigned u) { return __builtin_bit_cast(float, u << 16); }
__device__ __forceinline__ float bf_hi(unsigned u) { return __builtin_bit_cast(float, u & 0xFFFF0000u); }
__device__ __forceinline__ unsigned pk_bf(float lo, float hi) {
    f32x2 v = {lo, hi};
    return __builtin_bit_cast(unsigned, __builtin_convertvector(v, bf16x2));
}
__host__ __device__ __forceinline__ int pos2tok(int p) {
    const int l = p >> 3, t = (p >> 2) & 1, r = p & 3;
    const int w = l >> 6, g = (l >> 4) & 3, bl = l & 15;
    return 32 * (16 * w + 4 * g + r) + 16 * t + bl;
}

#define MFMA_OP "v_mfma_f32_16x16x32_bf16 "
template <int Q> __device__ __forceinline__ void mf_ra0(f32x4 &c, v4u b) {
    asm volatile(MFMA_OP "%0, a[%c2:%c3], %1, 0" : "=&v"(c) : "v"(b), "i"(4 * Q), "i"(4 * Q + 3));
}
template <int Q> __device__ __forceinline__ void mf_ra(f32x4 &c, v4u b) {
    asm volatile(MFMA_OP "%0, a[%c2:%c3], %1, %0" : "+v"(c) : "v"(b), "i"(4 * Q), "i"(4 * Q + 3));
}
template <int Q> __device__ __forceinline__ void mf_rb0(f32x4 &c, v4u a) {
    asm volatile(MFMA_OP "%0, %1, a[%c2:%c3], 0" : "=&v"(c) : "v"(a), "i"(4 * Q), "i"(4 * Q + 3));
}
template <int Q> __device__ __forceinline__ void mf_rb(f32x4 &c, v4u a) {
    asm volatile(MFMA_OP "%0, %1, a[%c2:%c3], %0" : "+v"(c) : "v"(a), "i"(4 * Q), "i"(4 * Q + 3));
}
__device__ __forceinline__ void mf_vv0(f32x4 &c, v4u a, v4u b) {
    asm volatile(MFMA_OP "%0, %1, %2, 0" : "=&v"(c) : "v"(a), "v"(b));
}
__device__ __forceinline__ void mf_vv(f32x4 &c, v4u a, v4u b) {
    asm volatile(MFMA_OP "%0, %1, %2, %0" : "+v"(c) : "v"(a), "v"(b));
}
__device__ __forceinline__ void mf_cplx2(f32x4 &c, v4u a0, v4u b0, v4u a1, v4u b1) {
    asm volatile("s_nop 1\n\t" MFMA_OP "%0, %1, %2, 0\n\t" MFMA_OP "%0, %3, %4, %0"
                 : "=&v"(c) : "v"(a0), "v"(b0), "v"(a1), "v"(b1));
}
__device__ __forceinline__ void mf_cplx4(f32x4 &c, v4u a0, v4u b0, v4u a1, v4u b1, v4u a2, v4u b2, v4u a3, v4u b3) {
    asm volatile("s_nop 1\n\t" MFMA_OP "%0, %1, %2, 0\n\t" MFMA_OP "%0, %3, %4, %0\n\t" MFMA_OP "%0, %5, %6, %0\n\t" MFMA_OP "%0, %7, %8, %0"
                 : "=&v"(c) : "v"(a0), "v"(b0), "v"(a1), "v"(b1), "v"(a2), "v"(b2), "v"(a3), "v"(b3));
}
#define MF_FENCE4(a, b, c, d) asm volatile("s_nop 7" : "+v"(a), "+v"(b), "+v"(c), "+v"(d))

template <int Q, int IMM> __device__ __forceinline__ void ring_ld(const GAS unsigned char *sbase, unsigned voff) {
    asm volatile("global_load_dwordx4 a[%c2:%c3], %0, %1 offset:%c4" ::"v"(voff), "s"(sbase), "i"(4 * Q), "i"(4 * Q + 3), "i"(IMM));
}
template <int Q> __device__ __forceinline__ void ring_ld_q(const GAS unsigned char *run, unsigned voff) {
    ring_ld<Q, (Q & 3) * 1024>(run + (Q >> 2) * 4096, voff);
}
template <int LO, int HI> __device__ __forceinline__ void ring_fill(const GAS unsigned char *run, unsigned voff) {
    static_for<LO, HI>([&](auto q) { ring_ld_q<q.value>(run, voff); });
}
#define DRAIN() asm volatile("s_waitcnt vmcnt(0)" ::: "memory")

__device__ __forceinline__ v4u ld_sc1(__amdgpu_buffer_rsrc_t rs, int voff, int soff) {
    return __builtin_amdgcn_raw_buffer_load_b128(rs, voff, soff, AUX_SC1);
}
__device__ __forceinline__ v4u ld_pl(__amdgpu_buffer_rsrc_t rs, int voff, int soff) {
    return __builtin_amdgcn_raw_buffer_load_b128(rs, voff, soff, 0);
}
__device__ __forceinline__ void st_sc1(v4u v, __amdgpu_buffer_rsrc_t rs, int voff, int soff) {
    __builtin_amdgcn_raw_buffer_store_b128(v, rs, voff + soff, 0, AUX_SC1);
}
__device__ __forceinline__ v4u lds_rd128(LAS unsigned char *L, int off) { return *(LAS v4u *)(L + off); }
__device__ __forceinline__ v2u lds_tr(LAS unsigned char *L, int off) {
    return __builtin_bit_cast(v2u, __builtin_amdgcn_ds_read_tr16_b64_v4i16((LAS s16x4 *)(L + off)));
}

struct Params {
    const uint16_t *x, *wr, *wi, *w1, *b1, *w2, *b2;
    uint16_t *out;
    unsigned char *ws;
};

__device__ __forceinline__ void signal_add(gu32 *word) {
    DRAIN();
    __syncthreads();
    if (threadIdx.x == 0) __hip_atomic_fetch_add(word, 1u, RLX_AGENT);
}
template <int NSH> __device__ __forceinline__ bool wait_words(gu32 *words, unsigned want, gu32 *tmo, LAS unsigned char *L) {
    volatile LAS unsigned *flag = (volatile LAS unsigned *)(L + L_FLAG);
    if (threadIdx.x < 64) {
        const int lane = threadIdx.x;
        unsigned ok = 1u;
        for (unsigned spins = 0;;) {
            unsigned v = want;
            if (lane < NSH) v = __hip_atomic_load(words + lane * 64, RLX_AGENT);
            if (__all(v >= want)) break;
            if (++spins > SPIN_CAP) {
                if (lane == 0) __hip_atomic_store(tmo, 1u, RLX_AGENT);
                ok = 0u;
                break;
            }
            if ((spins & 255u) == 0u && __hip_atomic_load(tmo, RLX_AGENT) != 0u) { ok = 0u; break; }
        }
        __builtin_amdgcn_fence(__ATOMIC_ACQUIRE, "wavefront");
        if (lane == 0) *flag = ok;
    }
    __syncthreads();
    return *flag != 0u;
}

struct CConst {
    v4u Cr[2], Ci[2], nCi[2];
    v4u Er[2], Ei[2], nEi[2];
    float t1r[8], t1i[8], t2r[8], t2i[8];
};
__device__ __forceinline__ void c_consts(CConst &K, int w, int lane) {
    const int g = lane >> 4, i = lane & 15;
#pragma unroll
    for (int ks = 0; ks < 2; ++ks) {
        float c[8], s[8];
#pragma unroll
        for (int j = 0; j < 8; ++j) {
            const float r = (float)(((32 * ks + 8 * g + j) * (16 * w + i)) & 63) * (1.0f / 64.0f);
            c[j] = __builtin_amdgcn_cosf(r);
            s[j] = __builtin_amdgcn_sinf(r);
        }
#pragma unroll
        for (int h = 0; h < 4; ++h) {
            K.Cr[ks][h] = pk_bf(c[2 * h], c[2 * h + 1]);
            K.nCi[ks][h] = pk_bf(s[2 * h], s[2 * h + 1]);
            K.Ci[ks][h] = K.nCi[ks][h] ^ 0x80008000u;
        }
    }
#pragma unroll
    for (int td = 0; td < 2; ++td) {
        float c[8], s[8];
#pragma unroll
        for (int j = 0; j < 8; ++j) {
            const float r = (float)(((16 * (j >> 2) + 4 * g + (j & 3)) * (16 * td + i)) & 31) * (1.0f / 32.0f);
            c[j] = __builtin_amdgcn_cosf(r);
            s[j] = __builtin_amdgcn_sinf(r);
        }
#pragma unroll
        for (int h = 0; h < 4; ++h) {
            K.Er[td][h] = pk_bf(c[2 * h], c[2 * h + 1]);
            K.nEi[td][h] = pk_bf(s[2 * h], s[2 * h + 1]);
            K.Ei[td][h] = K.nEi[td][h] ^ 0x80008000u;
        }
    }
#pragma unroll
    for (int e = 0; e < 8; ++e) {
        const int tb = e >> 2, r = e & 3;
        const float a1 = (float)(((16 * tb + 4 * g + r) * (16 * w + i)) & 2047) * (1.0f / 2048.0f);
        K.t1r[e] = __builtin_amdgcn_cosf(a1);
        K.t1i[e] = -__builtin_amdgcn_sinf(a1);
        const float a2 = (float)(((16 * tb + i) * (16 * w + 4 * g + r)) & 2047) * (1.0f / 2048.0f);
        K.t2r[e] = __builtin_amdgcn_cosf(a2) * (1.0f / 2048.0f);
        K.t2i[e] = __builtin_amdgcn_sinf(a2) * (1.0f / 2048.0f);
    }
}

__device__ __forceinline__ void cmul_pack(const f32x4 (&re)[2], const f32x4 (&im)[2], const float (&tr)[8], const float (&ti)[8], v4u &ore, v4u &oim) {
    float a[8], b[8];
#pragma unroll
    for (int e = 0; e < 8; ++e) {
        const float x = re[e >> 2][e & 3], y = im[e >> 2][e & 3];
        a[e] = x * tr[e] - y * ti[e];
        b[e] = x * ti[e] + y * tr[e];
    }
#pragma unroll
    for (int h = 0; h < 4; ++h) {
        ore[h] = pk_bf(a[2 * h], a[2 * h + 1]);
        oim[h] = pk_bf(b[2 * h], b[2 * h + 1]);
    }
}

template <bool SC1>
__device__ __forceinline__ void phase_c(const CConst &K, LAS unsigned char *L, __amdgpu_buffer_rsrc_t rs, int q, int layer, int w, int lane) {
    const int g = lane >> 4, bl = lane & 15;
    const unsigned xin = OFF_XH + (2 * layer) * XH_BYTES, xout = OFF_XH + (2 * layer + 1) * XH_BYTES;
    const int voff = (64 * w + lane) * 16;
    v4u xr[2], xi[2], wr[2], wi[2], ssv[4];
#pragma unroll
    for (int c = 0; c < 2; ++c) {
        xr[c] = __builtin_amdgcn_raw_buffer_load_b128(rs, voff, xin + (4 * q + 2 * c) * 4096, SC1 ? AUX_SC1 : 0);
        xi[c] = __builtin_amdgcn_raw_buffer_load_b128(rs, voff, xin + (4 * q + 2 * c + 1) * 4096, SC1 ? AUX_SC1 : 0);
    }
#pragma unroll
    for (int h = 0; h < 4; ++h)
        ssv[h] = __builtin_amdgcn_raw_buffer_load_b128(rs, (64 * w + lane) * 64 + h * 16, OFF_SS + layer * 16384, SC1 ? AUX_SC1 : 0);
#pragma unroll
    for (int c = 0; c < 2; ++c) {
        wr[c] = ld_pl(rs, voff, OFF_WT + (2 * q + c) * 8192);
        wi[c] = ld_pl(rs, voff, OFF_WT + (2 * q + c) * 8192 + 4096);
    }
    float rstd[8];
#pragma unroll
    for (int e = 0; e < 8; ++e) {
        const v2u u = {ssv[e >> 1][(e & 1) * 2], ssv[e >> 1][(e & 1) * 2 + 1]};
        const float ss = (float)__builtin_bit_cast(double, u);
        rstd[e] = __builtin_amdgcn_rsqf(ss * (1.0f / 640.0f) + 1e-5f);
    }
    const int wadr = bl * 128 + (((2 * w + (g >> 1)) ^ ((bl >> 1) & 7)) * 16) + (g & 1) * 8;
    const int radr0 = bl * 128 + ((g ^ ((bl >> 1) & 7)) * 16), radr1 = bl * 128 + (((4 + g) ^ ((bl >> 1) & 7)) * 16);
    const bool col0 = (q == 0);
#pragma unroll
    for (int c = 0; c < 2; ++c) {
        float zr[8], zi[8];
#pragma unroll
        for (int h = 0; h < 4; ++h) {
            zr[2 * h] = bf_lo(xr[c][h]) * rstd[2 * h];
            zr[2 * h + 1] = bf_hi(xr[c][h]) * rstd[2 * h + 1];
            zi[2 * h] = bf_lo(xi[c][h]) * rstd[2 * h];
            zi[2 * h + 1] = bf_hi(xi[c][h]) * rstd[2 * h + 1];
        }
        if (c == 0 && col0) {
#pragma unroll
            for (int e = 0; e < 8; ++e) zi[e] = 0.f;
        }
#pragma unroll
        for (int t = 0; t < 2; ++t) {
            const v2u a = {pk_bf(zr[4 * t], zr[4 * t + 1]), pk_bf(zr[4 * t + 2], zr[4 * t + 3])};
            const v2u b = {pk_bf(zi[4 * t], zi[4 * t + 1]), pk_bf(zi[4 * t + 2], zi[4 * t + 3])};
            *(LAS v2u *)(L + c * 8192 + t * 2048 + wadr) = a;
            *(LAS v2u *)(L + c * 8192 + 4096 + t * 2048 + wadr) = b;
        }
    }
    __syncthreads();
#pragma unroll
    for (int c = 0; c < 2; ++c) {
        f32x4 ur[2], ui[2];
#pragma unroll
        for (int tb = 0; tb < 2; ++tb) {
            const v4u r0 = lds_rd128(L, c * 8192 + tb * 2048 + radr0), r1 = lds_rd128(L, c * 8192 + tb * 2048 + radr1);
            const v4u i0 = lds_rd128(L, c * 8192 + 4096 + tb * 2048 + radr0), i1 = lds_rd128(L, c * 8192 + 4096 + tb * 2048 + radr1);
            mf_cplx4(ur[tb], r0, K.Cr[0], r1, K.Cr[1], i0, K.nCi[0], i1, K.nCi[1]);
            mf_cplx4(ui[tb], r0, K.Ci[0], r1, K.Ci[1], i0, K.Cr[0], i1, K.Cr[1]);
        }
        MF_FENCE4(ur[0], ur[1], ui[0], ui[1]);
        v4u vr, vi;
        cmul_pack(ur, ui, K.t1r, K.t1i, vr, vi);
        f32x4 Xr[2], Xi[2];
#pragma unroll
        for (int td = 0; td < 2; ++td) {
            mf_cplx2(Xr[td], K.Er[td], vr, K.nEi[td], vi);
            mf_cplx2(Xi[td], K.Ei[td], vr, K.Er[td], vi);
        }
        MF_FENCE4(Xr[0], Xr[1], Xi[0], Xi[1]);
        float fr[8], fi[8];
#pragma unroll
        for (int h = 0; h < 4; ++h) {
            fr[2 * h] = bf_lo(wr[c][h]); fr[2 * h + 1] = bf_hi(wr[c][h]);
            fi[2 * h] = bf_lo(wi[c][h]); fi[2 * h + 1] = bf_hi(wi[c][h]);
        }
        v4u yr, yi;
        cmul_pack(Xr, Xi, fr, fi, yr, yi);
        f32x4 pr[2], pi[2];
#pragma unroll
        for (int tb = 0; tb < 2; ++tb) {
            mf_cplx2(pr[tb], yr, K.Er[tb], yi, K.Ei[tb]);
            mf_cplx2(pi[tb], yr, K.nEi[tb], yi, K.Er[tb]);
        }
        MF_FENCE4(pr[0], pr[1], pi[0], pi[1]);
        v4u pre, pim;
        cmul_pack(pr, pi, K.t2r, K.t2i, pre, pim);
#pragma unroll
        for (int t = 0; t < 2; ++t) {
            *(LAS v2u *)(L + 16384 + c * 8192 + t * 2048 + wadr) = v2u{pre[2 * t], pre[2 * t + 1]};
            *(LAS v2u *)(L + 16384 + c * 8192 + 4096 + t * 2048 + wadr) = v2u{pim[2 * t], pim[2 * t + 1]};
        }
    }
    __syncthreads();
#pragma unroll
    for (int c = 0; c < 2; ++c) {
        f32x4 or_[2], oi[2];
#pragma unroll
        for (int tb = 0; tb < 2; ++tb) {
            const int base = 16384 + c * 8192 + tb * 2048;
            const v4u r0 = lds_rd128(L, base + radr0), r1 = lds_rd128(L, base + radr1);
            const v4u i0 = lds_rd128(L, base + 4096 + radr0), i1 = lds_rd128(L, base + 4096 + radr1);
            mf_cplx4(or_[tb], K.Cr[0], r0, K.Cr[1], r1, K.Ci[0], i0, K.Ci[1], i1);
            mf_cplx4(oi[tb], K.Cr[0], i0, K.Cr[1], i1, K.nCi[0], r0, K.nCi[1], r1);
        }
        MF_FENCE4(or_[0], or_[1], oi[0], oi[1]);
        v4u orr, oii;
#pragma unroll
        for (int h = 0; h < 4; ++h) {
            const int t = h >> 1, r = (h & 1) * 2;
            orr[h] = pk_bf(bf_lo(xr[c][h]) + or_[t][r], bf_hi(xr[c][h]) + or_[t][r + 1]);
            oii[h] = pk_bf(bf_lo(xi[c][h]) + oi[t][r], bf_hi(xi[c][h]) + oi[t][r + 1]);
        }
        if (c == 0 && col0) oii = xi[0];
        st_sc1(orr, rs, voff, xout + (4 * q + 2 * c) * 4096);
        st_sc1(oii, rs, voff, xout + (4 * q + 2 * c + 1) * 4096);
    }
}

__device__ __forceinline__ float gelu_f(float x) {
    const float t = x * (x * x * -0.10294324f + -2.3022082f);
    return x * __builtin_amdgcn_rcpf(1.0f + __builtin_amdgcn_exp2f(t));
}

template <bool LAST>
__device__ __forceinline__ void phase_g1(const Params &p, LAS unsigned char *L, __amdgpu_buffer_rsrc_t rs, int i, int j, int w, int lane,
                                         const GAS unsigned char *nxt, v4u (&resid)[3]) {
    const int g = lane >> 4, n = lane & 15;
    const unsigned xin = OFF_XH + (LAST ? 3 : 1) * XH_BYTES;
    {
        const int row = 8 * w + (lane >> 3), ch = lane & 7;
        const int voff = row * 4096 + i * 128 + ch * 16;
        const int ladr = row * 128 + (((ch >> 1) ^ ((row >> 1) & 3)) * 32) + (ch & 1) * 16;
        v4u t[20];
#pragma unroll
        for (int ks = 0; ks < 20; ++ks) t[ks] = ld_sc1(rs, voff, xin + ks * 32 * 4096);
#pragma unroll
        for (int ks = 0; ks < 20; ++ks) *(LAS v4u *)(L + ks * 4096 + ladr) = t[ks];
    }
    float b1v[12];
#pragma unroll
    for (int r = 0; r < 3; ++r) {
        const v2u u = *(const GAS v2u *)((const GAS unsigned char *)p.b1 + (192 * j + 48 * w + 16 * r + 4 * g) * 2);
        b1v[4 * r] = bf_lo(u.x); b1v[4 * r + 1] = bf_hi(u.x); b1v[4 * r + 2] = bf_lo(u.y); b1v[4 * r + 3] = bf_hi(u.y);
    }
    asm volatile("" : "+v"(b1v[0]), "+v"(b1v[1]), "+v"(b1v[2]), "+v"(b1v[3]), "+v"(b1v[4]), "+v"(b1v[5]), "+v"(b1v[6]), "+v"(b1v[7]),
                 "+v"(b1v[8]), "+v"(b1v[9]), "+v"(b1v[10]), "+v"(b1v[11]));
    DRAIN();
    __syncthreads();
    TS(LAST ? 17 : 5);
    int tra[4];
    {
        const int q4 = (lane >> 2) & 3, pi = lane & 3, row = 4 * g + q4;
#pragma unroll
        for (int u = 0; u < 4; ++u) tra[u] = row * 128 + ((((u + w) & 3) ^ ((row >> 1) & 3)) * 32) + pi * 8;
    }
    f32x4 acc[3][4], gram;
    v4u b[2][4];
    auto rd = [&](int ks, v4u(&o)[4]) {
#pragma unroll
        for (int u = 0; u < 4; ++u) {
            const v2u lo = lds_tr(L, tra[u] + ks * 4096), hi = lds_tr(L, tra[u] + ks * 4096 + 2048);
            o[u] = v4u{lo.x, lo.y, hi.x, hi.y};
        }
    };
    rd(0, b[0]);
    const unsigned voffr = lane * 16;
    static_for<0, 20>([&](auto kc) {
        constexpr int ks = kc.value;
        if constexpr (ks + 1 < 20) rd(ks + 1, b[(ks + 1) & 1]);
        static_for<0, 3>([&](auto rc) {
            constexpr int r = rc.value;
            static_for<0, 4>([&](auto uc) {
                constexpr int u = uc.value;
                if constexpr (ks == 0) mf_ra0<ks * 3 + r>(acc[r][u], b[ks & 1][u]);
                else mf_ra<ks * 3 + r>(acc[r][u], b[ks & 1][u]);
            });
            ring_ld_q<ks * 3 + r>(nxt, voffr);
        });
        if constexpr (ks == 0) {
            v4u m = b[0][0];
            if (g == 0) m.x &= 0x0000FFFFu;
            asm volatile("s_nop 1\n\t" MFMA_OP "%0, %1, %1, 0" : "=&v"(gram) : "v"(m));
        } else {
            asm volatile(MFMA_OP "%0, %1, %1, %0" : "+v"(gram) : "v"(b[ks & 1][0]));
        }
        __builtin_amdgcn_sched_barrier(0);
    });
    asm volatile("s_nop 7" : "+v"(gram), "+v"(acc[0][0]), "+v"(acc[0][1]), "+v"(acc[0][2]), "+v"(acc[0][3]), "+v"(acc[1][0]), "+v"(acc[1][1]),
                 "+v"(acc[1][2]), "+v"(acc[1][3]), "+v"(acc[2][0]), "+v"(acc[2][1]), "+v"(acc[2][2]), "+v"(acc[2][3]));
    TS(LAST ? 18 : 6);
    {
        const int r = n & 3;
        const float d = r == 0 ? gram[0] : r == 1 ? gram[1] : r == 2 ? gram[2] : gram[3];
        if (g == (n >> 2)) ((LAS float *)(L + L_SS))[16 * w + n] = d;
    }
    if (!LAST) {
#pragma unroll
        for (int rd_ = 0; rd_ < 3; ++rd_) {
            int it = (rd_ * 4 + w) * 64 + lane;
            if (it >= 640) it = lane;
            const int row = 80 * j + (it >> 3), ch = it & 7;
            resid[rd_] = lds_rd128(L, row * 128 + (((ch >> 1) ^ ((row >> 1) & 3)) * 32) + (ch & 1) * 16);
        }
    }
    __syncthreads();
    float rstd[4];
#pragma unroll
    for (int u = 0; u < 4; ++u)
        rstd[u] = __builtin_amdgcn_rsqf(((LAS float *)(L + L_SS))[16 * ((u + w) & 3) + n] * (1.0f / 640.0f) + 1e-5f);
    const unsigned hoff = OFF_H + (LAST ? 1 : 0) * H_BYTES + i * (48 * 4096);
    v2u hp[3][4];
#pragma unroll
    for (int r = 0; r < 3; ++r)
#pragma unroll
        for (int u = 0; u < 4; ++u) {
            float y[4];
#pragma unroll
            for (int e = 0; e < 4; ++e) y[e] = gelu_f(acc[r][u][e] * rstd[u] + b1v[4 * r + e]);
            hp[r][u] = v2u{pk_bf(y[0], y[1]), pk_bf(y[2], y[3])};
        }
    const int ft0 = 12 * j + 3 * w;
    if ((w & 1) == 0) {
#pragma unroll
        for (int u = 0; u < 4; ++u) {
            const int tt = (u + w) & 3;
            st_sc1(v4u{hp[0][u].x, hp[0][u].y, hp[1][u].x, hp[1][u].y}, rs, lane * 16, hoff + (ft0 >> 1) * 4096 + tt * 1024);
            __builtin_amdgcn_raw_buffer_store_b64(hp[2][u], rs, lane * 16, hoff + ((ft0 + 2) >> 1) * 4096 + tt * 1024, AUX_SC1);
        }
    } else {
#pragma unroll
        for (int u = 0; u < 4; ++u) {
            const int tt = (u + w) & 3;
            __builtin_amdgcn_raw_buffer_store_b64(hp[0][u], rs, lane * 16 + 8, hoff + (ft0 >> 1) * 4096 + tt * 1024, AUX_SC1);
            st_sc1(v4u{hp[1][u].x, hp[1][u].y, hp[2][u].x, hp[2][u].y}, rs, lane * 16, hoff + ((ft0 + 1) >> 1) * 4096 + tt * 1024);
        }
    }
}

__device__ __forceinline__ void phase_r(LAS unsigned char *L, __amdgpu_buffer_rsrc_t rs, int j, int w, int lane, f32x4 (&acc)[5][4]) {
    const int g = lane >> 4;
    int tra[4];
    {
        const int q4 = (lane >> 2) & 3, pi = lane & 3, row = 4 * g + q4;
#pragma unroll
        for (int u = 0; u < 4; ++u) tra[u] = (5 * w) * 4096 + row * 128 + ((u ^ ((row >> 1) & 3)) * 32) + pi * 8;
    }
    v4u a[25];
#pragma unroll
    for (int f = 0; f < 25; ++f) a[f] = ld_pl(rs, lane * 16, OFF_RB2 + ((j * 4 + w) * 25 + f) * 1024);
#pragma unroll
    for (int ks = 0; ks < 5; ++ks) {
        v4u b[4];
#pragma unroll
        for (int u = 0; u < 4; ++u) {
            const v2u lo = lds_tr(L, tra[u] + ks * 4096), hi = lds_tr(L, tra[u] + ks * 4096 + 2048);
            b[u] = v4u{lo.x, lo.y, hi.x, hi.y};
        }
#pragma unroll
        for (int m = 0; m < 5; ++m)
#pragma unroll
            for (int u = 0; u < 4; ++u) {
                if (ks == 0) mf_vv0(acc[m][u], a[ks * 5 + m], b[u]);
                else mf_vv(acc[m][u], a[ks * 5 + m], b[u]);
            }
    }
}

template <bool LAST>
__device__ __forceinline__ void phase_g2(const Params &p, LAS unsigned char *L, __amdgpu_buffer_rsrc_t rs, int i, int j, int w, int lane,
                                         f32x4 (&acc)[5][4], const v4u (&resid)[3]) {
    const int g = lane >> 4, n = lane & 15;
    const unsigned hoff = OFF_H + (LAST ? 1 : 0) * H_BYTES + i * (48 * 4096) + (12 * w) * 4096;
    v4u h[3][4];
#pragma unroll
    for (int u = 0; u < 4; ++u) {
        h[0][u] = ld_sc1(rs, lane * 16, hoff + u * 1024);
        h[1][u] = ld_sc1(rs, lane * 16, hoff + 4096 + u * 1024);
    }
    static_for<0, 12>([&](auto kc) {
        constexpr int ks = kc.value;
        if constexpr (ks + 2 < 12) {
#pragma unroll
            for (int u = 0; u < 4; ++u) h[(ks + 2) % 3][u] = ld_sc1(rs, lane * 16, hoff + (ks + 2) * 4096 + u * 1024);
        }
        static_for<0, 5>([&](auto mc) {
            constexpr int m = mc.value;
            static_for<0, 4>([&](auto uc) {
                constexpr int u = uc.value;
                if constexpr (LAST) mf_ra<ks * 5 + m>(acc[m][u], h[ks % 3][u]);
                else if constexpr (ks == 0) mf_rb0<ks * 5 + m>(acc[m][u], h[ks % 3][u]);
                else mf_rb<ks * 5 + m>(acc[m][u], h[ks % 3][u]);
            });
        });
        __builtin_amdgcn_sched_barrier(0);
    });
    asm volatile("s_nop 7" : "+v"(acc[0][0]), "+v"(acc[0][1]), "+v"(acc[0][2]), "+v"(acc[0][3]), "+v"(acc[1][0]), "+v"(acc[1][1]), "+v"(acc[1][2]),
                 "+v"(acc[1][3]), "+v"(acc[2][0]), "+v"(acc[2][1]), "+v"(acc[2][2]), "+v"(acc[2][3]));
    asm volatile("s_nop 7" : "+v"(acc[3][0]), "+v"(acc[3][1]), "+v"(acc[3][2]), "+v"(acc[3][3]), "+v"(acc[4][0]), "+v"(acc[4][1]), "+v"(acc[4][2]),
                 "+v"(acc[4][3]));
    TS(LAST ? 23 : 10);
    if (!LAST) {
#pragma unroll
        for (int m = 0; m < 5; ++m)
#pragma unroll
            for (int u = 0; u < 4; ++u) *(LAS f32x4 *)(L + w * L_PART + (16 * m + n) * 272 + (16 * u + 4 * g) * 4) = acc[m][u];
        __syncthreads();
        const unsigned xout = OFF_XH + 2 * XH_BYTES;
        float ssq[8];
#pragma unroll
        for (int e = 0; e < 8; ++e) ssq[e] = 0.f;
#pragma unroll
        for (int rd_ = 0; rd_ < 3; ++rd_) {
            const int it = (rd_ * 4 + w) * 64 + lane;
            if (rd_ < 2 || w < 2) {
                const int row = it >> 3, ch = it & 7;
                f32x4 s0 = {0.f, 0.f, 0.f, 0.f}, s1 = {0.f, 0.f, 0.f, 0.f};
#pragma unroll
                for (int ww = 0; ww < 4; ++ww) {
                    s0 += *(LAS f32x4 *)(L + ww * L_PART + row * 272 + ch * 32);
                    s1 += *(LAS f32x4 *)(L + ww * L_PART + row * 272 + ch * 32 + 16);
                }
                const int kp = 80 * j + row;
                const float bb = *(const GAS float *)((const GAS unsigned char *)p.ws + OFF_B2P + kp * 4);
                float v[8];
#pragma unroll
                for (int e = 0; e < 8; ++e) {
                    const unsigned u = resid[rd_][e >> 1];
                    v[e] = ((e & 1) ? bf_hi(u) : bf_lo(u)) + (e < 4 ? s0[e & 3] : s1[e & 3]) + bb;
                }
                const v4u o = {pk_bf(v[0], v[1]), pk_bf(v[2], v[3]), pk_bf(v[4], v[5]), pk_bf(v[6], v[7])};
                st_sc1(o, rs, kp * 4096 + i * 128 + ch * 16, xout);
                if (kp != 1) {
#pragma unroll
                    for (int e = 0; e < 8; ++e) {
                        const float z = (e & 1) ? bf_hi(o[e >> 1]) : bf_lo(o[e >> 1]);
                        ssq[e] += z * z;
                    }
                }
            }
        }
#pragma unroll
        for (int e = 0; e < 8; ++e) {
            ssq[e] += __shfl_xor(ssq[e], 8);
            ssq[e] += __shfl_xor(ssq[e], 16);
            ssq[e] += __shfl_xor(ssq[e], 32);
        }
        if (lane < 8) {
            *(LAS f32x4 *)(L + L_SSP + w * 256 + lane * 32) = f32x4{ssq[0], ssq[1], ssq[2], ssq[3]};
            *(LAS f32x4 *)(L + L_SSP + w * 256 + lane * 32 + 16) = f32x4{ssq[4], ssq[5], ssq[6], ssq[7]};
        }
        __syncthreads();
        if (w == 0) {
            const LAS float *sp = (const LAS float *)(L + L_SSP);
            const float t = (sp[lane] + sp[64 + lane]) + (sp[128 + lane] + sp[192 + lane]);
            __hip_atomic_fetch_add((GAS double *)((GAS unsigned char *)p.ws + OFF_SS + 16384) + 64 * i + lane, (double)t, RLX_AGENT);
        }
    } else {
#pragma unroll
        for (int m = 0; m < 5; ++m)
#pragma unroll
            for (int u = 0; u < 4; ++u) *(LAS f32x4 *)(L + w * L_PART + (16 * u + n) * 336 + (16 * m + 4 * g) * 4) = acc[m][u];
        __syncthreads();
#pragma unroll
        for (int rd_ = 0; rd_ < 3; ++rd_) {
            const int it = (rd_ * 4 + w) * 64 + lane;
            if (rd_ < 2 || w < 2) {
                const int tok = it / 10, ch = it - tok * 10;
                f32x4 s0 = {0.f, 0.f, 0.f, 0.f}, s1 = {0.f, 0.f, 0.f, 0.f};
#pragma unroll
                for (int ww = 0; ww < 4; ++ww) {
                    s0 += *(LAS f32x4 *)(L + ww * L_PART + tok * 336 + ch * 32);
                    s1 += *(LAS f32x4 *)(L + ww * L_PART + tok * 336 + ch * 32 + 16);
                }
                const int d0 = 80 * j + 8 * ch;
                const v4u bb = *(const GAS v4u *)((const GAS unsigned char *)p.b2 + d0 * 2);
                float v[8];
#pragma unroll
                for (int e = 0; e < 8; ++e) v[e] = (e < 4 ? s0[e & 3] : s1[e & 3]) + ((e & 1) ? bf_hi(bb[e >> 1]) : bf_lo(bb[e >> 1]));
                const v4u o = {pk_bf(v[0], v[1]), pk_bf(v[2], v[3]), pk_bf(v[4], v[5]), pk_bf(v[6], v[7])};
                *(GAS v4u *)((GAS unsigned char *)p.out + pos2tok(64 * i + tok) * 1280 + d0 * 2) = o;
            }
        }
    }
}

template <int PB, int PE> __global__ void __launch_bounds__(256, 1) k_main(Params p) {
    extern __shared__ __attribute__((aligned(16))) unsigned char smem[];
    LAS unsigned char *L = (LAS unsigned char *)smem;
    asm volatile("" ::: "a239");
    const __amdgpu_buffer_rsrc_t rs = __builtin_amdgcn_make_buffer_rsrc((void *)p.ws, 0, (int)WS_USED, RSRC_W3);
    const int lane = threadIdx.x & 63, w = __builtin_amdgcn_readfirstlane(threadIdx.x >> 6);
    const int bid = blockIdx.x, slot = bid >> 3;
    const int i = 4 * (bid & 7) + (slot >> 3), j = slot & 7;
    gu32 *ctl = (gu32 *)(p.ws + OFF_CTRL);
    const GAS unsigned char *wsg = (const GAS unsigned char *)p.ws;
    const GAS unsigned char *run1 = wsg + OFF_W1F + (j * 4 + w) * 61440;
    const GAS unsigned char *run2 = wsg + OFF_W2F + (j * 4 + w) * 61440;
    const GAS unsigned char *run3 = wsg + OFF_W2T + (j * 4 + w) * 61440;
    const unsigned voffr = lane * 16;
    const bool isC = bid < 160;
    TS(0);

    if constexpr (PB == 0) {
        if (isC) {
            CConst K;
            c_consts(K, w, lane);
            TS(1);
            phase_c<false>(K, L, rs, bid, 0, w, lane);
            TS(2);
        }
        if constexpr (PE > 1) {
            if (isC) signal_add(ctl + CW_BAR + (0 * 16 + (bid & 15)) * 64);
            TS(3);
        }
    }
    if constexpr (PB <= 1 && PE > 1) {
        ring_fill<0, 60>(run1, voffr);
        if constexpr (PB == 0) {
            if (!wait_words<16>(ctl + CW_BAR + 0 * 16 * 64, 10u, ctl + CW_TMO, L)) return;
        }
        TS(4);
        v4u resid[3];
        phase_g1<false>(p, L, rs, i, j, w, lane, run2, resid);
        TS(7);
        signal_add(ctl + CW_GRP + i * 64);
        TS(8);
        if (!wait_words<1>(ctl + CW_GRP + i * 64, 8u, ctl + CW_TMO, L)) return;
        TS(9);
        f32x4 acc[5][4];
        phase_g2<false>(p, L, rs, i, j, w, lane, acc, resid);
        TS(11);
        if constexpr (PE > 2) signal_add(ctl + CW_BAR + (1 * 16 + (bid & 15)) * 64);
        TS(12);
    }
    if constexpr (PB <= 2 && PE > 2) {
        if (isC) {
            CConst K;
            c_consts(K, w, lane);
            if constexpr (PB < 2) {
                if (!wait_words<16>(ctl + CW_BAR + 1 * 16 * 64, 16u, ctl + CW_TMO, L)) return;
            }
            TS(13);
            phase_c<true>(K, L, rs, bid, 1, w, lane);
            TS(14);
            if constexpr (PE > 3) signal_add(ctl + CW_BAR + (2 * 16 + (bid & 15)) * 64);
            TS(15);
        }
    }
    if constexpr (PE > 3) {
        ring_fill<0, 60>(run1, voffr);
        if constexpr (PB < 3) {
            if (!wait_words<16>(ctl + CW_BAR + 2 * 16 * 64, 10u, ctl + CW_TMO, L)) return;
        }
        TS(16);
        v4u resid[3];
        phase_g1<true>(p, L, rs, i, j, w, lane, run3, resid);
        TS(19);
        signal_add(ctl + CW_GRP + (32 + i) * 64);
        TS(20);
        f32x4 acc[5][4];
        phase_r(L, rs, j, w, lane, acc);
        TS(21);
        if (!wait_words<1>(ctl + CW_GRP + (32 + i) * 64, 8u, ctl + CW_TMO, L)) return;
        TS(22);
        phase_g2<true>(p, L, rs, i, j, w, lane, acc, resid);
        TS(24);
    }
    TS_DUMP();
}

__device__ __forceinline__ int col_bin(int c) {
    const int k1 = c >> 5, k2 = c & 31;
    const int a = k2 < 16 ? k1 + 20 * k2 : 640 - k1 - 20 * k2;
    return c < 16 ? 20 * c : c < 32 ? 10 + 20 * (c - 16) : a;
}
__device__ __forceinline__ float kap_bin(int kap) { return kap == 0 ? 320.0f : (float)col_bin(kap >> 1); }
__device__ __forceinline__ float kap_phase(int kap) { return (kap > 1 && (kap & 1)) ? 0.25f : 0.0f; }

constexpr int col_bin_c(int c) {
    return c < 16 ? 20 * c : c < 32 ? 10 + 20 * (c - 16) : (c & 31) < 16 ? (c >> 5) + 20 * (c & 31) : 640 - (c >> 5) - 20 * (c & 31);
}
constexpr int bin_col_c(int k) {
    return k == 320 ? 0 : k % 20 == 0 ? k / 20 : k % 20 == 10 ? 16 + k / 20 : k % 20 < 10 ? 32 * (k % 20) + k / 20 : 32 * (20 - k % 20) + 31 - k / 20;
}
constexpr double cos_small(double x) {
    double t = 1.0, s = 1.0;
    for (int i = 1; i <= 9; ++i) { t *= -x * x / ((2 * i - 1) * (2 * i)); s += t; }
    return s;
}
constexpr double sin_small(double x) {
    double t = x, s = x;
    for (int i = 1; i <= 9; ++i) { t *= -x * x / ((2 * i) * (2 * i + 1)); s += t; }
    return s;
}
struct Cos640 { double v[640]; };
constexpr Cos640 make_cos640() {
    Cos640 c = {};
    for (int m = 0; m < 640; ++m) {
        const int a = m <= 320 ? m : 640 - m;
        const bool neg = a > 160;
        const int b = neg ? 320 - a : a;
        const double v = b <= 80 ? cos_small(b * (3.14159265358979323846 / 320.0)) : sin_small((160 - b) * (3.14159265358979323846 / 320.0));
        c.v[m] = neg ? -v : v;
    }
    return c;
}
constexpr uint16_t bf16_c(double d) {
    const unsigned u = __builtin_bit_cast(unsigned, (float)d);
    return (uint16_t)((u + 0x7FFFu + ((u >> 16) & 1u)) >> 16);
}
struct PrepTab {
    uint16_t b1[40][64][8];
    uint16_t f20[2][64][8];
    uint16_t cmap[40][8];
};
constexpr PrepTab make_prep_tab() {
    const Cos640 c = make_cos640();
    PrepTab t = {};
    for (int tau = 0; tau < 40; ++tau)
        for (int l = 0; l < 64; ++l) {
            const int kap = 16 * tau + (l & 15), g = l >> 4;
            const int bin = kap == 0 ? 320 : col_bin_c(kap >> 1), ph = (kap > 1 && (kap & 1)) ? 160 : 0;
            const double sc = kap < 2 ? 0.70710678118654752 : 1.0;
            for (int j = 0; j < 8; ++j) t.b1[tau][l][j] = bf16_c(sc * c.v[((16 * (j >> 2) + 4 * g + (j & 3)) * bin + ph) % 640]);
        }
    for (int u = 0; u < 2; ++u)
        for (int l = 0; l < 64; ++l) {
            const int s = 16 * u + (l & 15), g = l >> 4;
            const int k1 = s == 0 ? 0 : s == 1 ? 10 : s >> 1, ph = (s > 1 && (s & 1)) ? 160 : 0;
            for (int j = 0; j < 8; ++j) {
                const int d1 = j < 4 ? 4 * g + j : 12 + 4 * g + j;
                t.f20[u][l][j] = (s < 20 && d1 < 20) ? bf16_c(0.055901699437494742 * c.v[(32 * d1 * k1 + ph) % 640]) : (uint16_t)0;
            }
        }
    for (int cg = 0; cg < 40; ++cg)
        for (int e = 0; e < 8; ++e) t.cmap[cg][e] = (uint16_t)bin_col_c(1 + 8 * cg + e);
    return t;
}
static __device__ const __attribute__((aligned(16))) PrepTab g_prep_tab = make_prep_tab();

#define TSA(n)
constexpr int A_T = 0;
constexpr int A_Y = 41216;
constexpr int A_RED = A_Y + 41600;
constexpr int A_BYTES = 84 * 1024;
constexpr int N_X = 64, N_W1 = 48, N_W2 = 48, N_RB = 20, N_WT = 32, N_W2T = 24;
constexpr int B_W1 = N_X, B_W2 = B_W1 + N_W1, B_B2 = B_W2 + N_W2, B_RB = B_B2 + 1, B_WT = B_RB + N_RB, B_W2T = B_WT + N_WT, A_GRID = B_W2T + N_W2T;

__device__ __forceinline__ f32x4 mfma16(v4u a, v4u b, f32x4 c) {
    return __builtin_amdgcn_mfma_f32_16x16x32_bf16(__builtin_bit_cast(bf16x8, a), __builtin_bit_cast(bf16x8, b), c, 0, 0, 0);
}
#define PREP_AUX AUX_SC1
__device__ __forceinline__ void st_ws(v4u v, __amdgpu_buffer_rsrc_t rs, int voff, int soff) {
    __builtin_amdgcn_raw_buffer_store_b128(v, rs, voff + soff, 0, PREP_AUX);
}

template <int KIND>
__device__ __forceinline__ void dft_task(const Params &p, LAS unsigned char *L, __amdgpu_buffer_rsrc_t rs, int task, int w, int lane) {
    const int g = lane >> 4, n = lane & 15;
    const GAS unsigned char *tb = (const GAS unsigned char *)&g_prep_tab;
    v4u f20[2], b1[5][2], b2[5][2];
#pragma unroll
    for (int u = 0; u < 2; ++u) f20[u] = *(const GAS v4u *)(tb + 40960 + u * 1024 + lane * 16);
#pragma unroll
    for (int b = 0; b < 5; ++b)
#pragma unroll
        for (int u = 0; u < 2; ++u) b1[b][u] = *(const GAS v4u *)(tb + (2 * (5 * w + b) + u) * 1024 + lane * 16);
    v4u ld[KIND == 1 ? 10 : 12];
    if constexpr (KIND == 1) {
#pragma unroll
        for (int i = 0; i < 10; ++i) {
            const int rp = 16 * w + 64 * i + (lane >> 2), d2 = (rp * 3277) >> 16, d1 = rp - 20 * d2;
            ld[i] = *(const GAS v4u *)((const GAS unsigned char *)p.w1 + (32 * d1 + d2) * 3072 + task * 64 + (lane & 3) * 16);
        }
    } else {
        const int rq = lane & 7, cc = lane >> 3;
        const GAS unsigned char *rowp[4];
#pragma unroll
        for (int e = 0; e < 4; ++e) {
            if constexpr (KIND == 0) rowp[e] = (const GAS unsigned char *)p.x + pos2tok(32 * task + 8 * (rq & 3) + 4 * (rq >> 2) + e) * 1280;
            else if constexpr (KIND == 2) rowp[e] = (const GAS unsigned char *)p.w2 + (32 * task + 4 * rq + e) * 1280;
            else rowp[e] = (const GAS unsigned char *)p.b2;
        }
#pragma unroll
        for (int i = 0; i < 3; ++i) {
            const int cg = (i < 2 || w < 2) ? w + 4 * i : w;
#pragma unroll
            for (int e = 0; e < 4; ++e) ld[4 * i + e] = *(const GAS v4u *)(rowp[e] + (8 * cg + cc) * 16);
        }
    }
    __builtin_amdgcn_sched_barrier(0);
    TSA(1);
#pragma unroll
    for (int b = 0; b < 5; ++b)
#pragma unroll
        for (int u = 0; u < 2; ++u) {
            const int ks = 5 * w + b;
            const unsigned sg = (((ks & 1) ^ (n & 1) ^ 1) ? 0x80008000u : 0u);
#pragma unroll
            for (int h = 0; h < 4; ++h) {
                if (KIND == 1 && b == 0 && u == 0 && w == 0 && n == 1) b1[b][u][h] = 0u;
                const unsigned o = (unsigned)__builtin_amdgcn_update_dpp(0, (int)b1[b][u][h], 0xB1, 0xF, 0xF, true);
                b2[b][u][h] = ks < 2 ? 0u : o ^ sg;
            }
            asm volatile("" : "+v"(b2[b][u]));
        }
    __builtin_amdgcn_sched_barrier(0);
    TSA(2);
    if constexpr (KIND == 1) {
#pragma unroll
        for (int i = 0; i < 10; ++i) {
            const int rp = 16 * w + 64 * i + (lane >> 2), d2 = (rp * 3277) >> 16, rho = rp + (d2 >> 3), ch = lane & 3;
            *(LAS v4u *)(L + A_T + rho * 64 + (((ch >> 1) ^ ((rho >> 2) & 1)) * 32) + (ch & 1) * 16) = ld[i];
        }
    } else {
        const int rq = lane & 7, cc = lane >> 3;
#pragma unroll
        for (int i = 0; i < 3; ++i) {
            if (i < 2 || w < 2) {
                const int c = 8 * (w + 4 * i) + cc, rho0 = 161 * (c & 3) + (c >> 2);
                const int a0 = A_T + rho0 * 64 + (((rq >> 2) ^ ((rho0 >> 2) & 1)) * 32) + (rq & 3) * 8, a1 = a0 ^ 32;
#pragma unroll
                for (int e = 0; e < 8; ++e) {
                    const unsigned sel = (e & 1) ? 0x07060302u : 0x05040100u;
                    const v2u o = {__builtin_amdgcn_perm(ld[4 * i + 1][e >> 1], ld[4 * i][e >> 1], sel),
                                   __builtin_amdgcn_perm(ld[4 * i + 3][e >> 1], ld[4 * i + 2][e >> 1], sel)};
                    *(LAS v2u *)(L + ((e & 1) ? a1 : a0) + e * 1280) = o;
                }
            }
        }
    }
    TSA(3);
    __syncthreads();
    TSA(4);
    const int q4 = (lane >> 2) & 3, pi = lane & 3;
    {
        const int rl = 161 * w + 4 * g + q4, rh = 161 * w + 16 + q4;
        const int al = A_T + rl * 64 + ((rl >> 2) & 1) * 32 + pi * 8, ah = A_T + rh * 64 + ((rh >> 2) & 1) * 32 + pi * 8;
        const int yw = A_Y + n * 2080 + 8 * w * 64 + g * 8;
        v4u a[8][2];
#pragma unroll
        for (int e = 0; e < 8; ++e)
#pragma unroll
            for (int t = 0; t < 2; ++t) {
                const int x = ((e + t) & 1) * 32;
                const v2u lo = lds_tr(L, (al ^ x) + e * 1280), hi = lds_tr(L, (ah ^ x) + e * 1280);
                a[e][t] = v4u{lo.x, lo.y, hi.x, hi.y};
            }
        v2u lo16[8][2], hi4[8][2];
#pragma unroll
        for (int e = 0; e < 8; ++e)
#pragma unroll
            for (int t = 0; t < 2; ++t) {
                const f32x4 z = {0.f, 0.f, 0.f, 0.f};
                const f32x4 y0 = mfma16(a[e][t], f20[0], z), y1 = mfma16(a[e][t], f20[1], z);
                lo16[e][t] = v2u{pk_bf(y0[0], y0[1]), pk_bf(y0[2], y0[3])};
                hi4[e][t] = v2u{pk_bf(y1[0], y1[1]), pk_bf(y1[2], y1[3])};
            }
#pragma unroll
        for (int e = 0; e < 8; ++e)
#pragma unroll
            for (int t = 0; t < 2; ++t) *(LAS v2u *)(L + yw + e * 64 + ((t ^ ((e >> 2) & 1)) * 32)) = lo16[e][t];
        if (n < 4) {
#pragma unroll
            for (int e = 0; e < 8; ++e)
#pragma unroll
                for (int t = 0; t < 2; ++t) *(LAS v2u *)(L + yw + 16 * 2080 + e * 64 + ((t ^ ((e >> 2) & 1)) * 32)) = hi4[e][t];
        }
    }
    TSA(5);
    __syncthreads();
    TSA(6);
    float ssq[8];
#pragma unroll
    for (int e = 0; e < 8; ++e) ssq[e] = 0.f;
    const int yr0 = A_Y + (4 * g + q4) * 64 + (g & 1) * 32 + pi * 8;
    v4u yra[5][2], yia[5][2];
#pragma unroll
    for (int b = 0; b < 5; ++b) {
        const int ks = 5 * w + b, sr = ks < 2 ? ks : ks & ~1, si = ks | 1;
#pragma unroll
        for (int t = 0; t < 2; ++t) {
            const int ar = (yr0 ^ (t * 32)) + sr * 2080, ai = (yr0 ^ (t * 32)) + si * 2080;
            const v2u rl = lds_tr(L, ar), rh = lds_tr(L, ar + 1024), il = lds_tr(L, ai), ih = lds_tr(L, ai + 1024);
            yra[b][t] = v4u{rl.x, rl.y, rh.x, rh.y};
            yia[b][t] = v4u{il.x, il.y, ih.x, ih.y};
        }
    }
#pragma unroll
    for (int b = 0; b < 5; ++b) {
        const int ks = 5 * w + b;
        const v4u(&yr)[2] = yra[b];
        const v4u(&yi)[2] = yia[b];
        f32x4 o[2][2];
        const f32x4 z = {0.f, 0.f, 0.f, 0.f};
#pragma unroll
        for (int u = 0; u < 2; ++u)
#pragma unroll
            for (int t = 0; t < 2; ++t) {
                if constexpr (KIND == 1) o[u][t] = mfma16(b2[b][u], yi[t], mfma16(b1[b][u], yr[t], z));
                else o[u][t] = mfma16(yi[t], b2[b][u], mfma16(yr[t], b1[b][u], z));
            }
        if constexpr (KIND == 1) {
#pragma unroll
            for (int t = 0; t < 2; ++t) {
                const int ft = 2 * task + t, j = ft / 12, r3 = ft - 12 * j, ww = r3 / 3, r = r3 - 3 * ww;
                const v4u v = {pk_bf(o[0][t][0], o[0][t][1]), pk_bf(o[0][t][2], o[0][t][3]), pk_bf(o[1][t][0], o[1][t][1]), pk_bf(o[1][t][2], o[1][t][3])};
                st_ws(v, rs, lane * 16, OFF_W1F + ((j * 4 + ww) * 60 + ks * 3 + r) * 1024);
            }
        } else {
#pragma unroll
            for (int u = 0; u < 2; ++u) {
                const int tau = 2 * ks + u;
                const v4u v = {pk_bf(o[u][0][0], o[u][0][1]), pk_bf(o[u][0][2], o[u][0][3]), pk_bf(o[u][1][0], o[u][1][1]), pk_bf(o[u][1][2], o[u][1][3])};
                if constexpr (KIND == 0) {
                    st_ws(v, rs, n * 4096 + g * 16, OFF_XH + tau * 16 * 4096 + task * 64);
                    const bool dc = (b == 0 && u == 0) && w == 0 && n == 1;
#pragma unroll
                    for (int e = 0; e < 8; ++e) {
                        const float z1 = dc ? 0.f : o[u][e >> 2][e & 3];
                        ssq[e] += z1 * z1;
                    }
                } else if constexpr (KIND == 2) {
                    const int j = tau / 5, m = tau - 5 * j, ww = task / 12, kf = task - 12 * ww;
                    st_ws(v, rs, lane * 16, OFF_W2F + ((j * 4 + ww) * 60 + kf * 5 + m) * 1024);
                } else {
                    if (g == 0) ((GAS float *)(p.ws + OFF_B2P))[16 * tau + n] = o[u][0][0];
                }
            }
        }
    }
    if constexpr (KIND == 0) {
#pragma unroll
        for (int e = 0; e < 8; ++e) {
            float v = ssq[e];
            v += __builtin_bit_cast(float, __builtin_amdgcn_update_dpp(0, __builtin_bit_cast(int, v), 0xB1, 0xF, 0xF, true));
            v += __builtin_bit_cast(float, __builtin_amdgcn_update_dpp(0, __builtin_bit_cast(int, v), 0x4E, 0xF, 0xF, true));
            v += __builtin_bit_cast(float, __builtin_amdgcn_update_dpp(0, __builtin_bit_cast(int, v), 0x141, 0xF, 0xF, true));
            v += __builtin_bit_cast(float, __builtin_amdgcn_update_dpp(0, __builtin_bit_cast(int, v), 0x140, 0xF, 0xF, true));
            ssq[e] = v;
        }
        LAS float *red = (LAS float *)(L + A_RED);
        if (n == 0) {
            *(LAS f32x4 *)(red + 32 * w + 8 * g) = f32x4{ssq[0], ssq[1], ssq[2], ssq[3]};
            *(LAS f32x4 *)(red + 32 * w + 8 * g + 4) = f32x4{ssq[4], ssq[5], ssq[6], ssq[7]};
        }
        __syncthreads();
        if (threadIdx.x < 32) {
            const int t = threadIdx.x;
            const float s = (red[t] + red[32 + t]) + (red[64 + t] + red[96 + t]);
            ((GAS double *)(p.ws + OFF_SS))[32 * task + t] = (double)s;
        }
    }
}

__device__ __forceinline__ void rb2_task(__amdgpu_buffer_rsrc_t rs, int ks, int w, int lane) {
    const int g = lane >> 4, n = lane & 15;
    float kb[8], ph[8];
#pragma unroll
    for (int jj = 0; jj < 8; ++jj) {
        const int kap = 32 * ks + 16 * (jj >> 2) + 4 * g + (jj & 3);
        kb[jj] = kap_bin(kap);
        ph[jj] = kap_phase(kap);
    }
    const float sc01 = (ks == 0 && g == 0) ? 0.039528471f : 0.055901699f;
#pragma unroll
    for (int jh = 0; jh < 2; ++jh)
#pragma unroll
        for (int m = 0; m < 5; ++m) {
            const int j = 2 * w + jh;
            const float d = (float)(80 * j + 16 * m + n);
            float c[8];
#pragma unroll
            for (int jj = 0; jj < 8; ++jj)
                c[jj] = __builtin_amdgcn_cosf(__builtin_amdgcn_fractf(kb[jj] * d * (1.0f / 640.0f)) + ph[jj]) * (jj < 2 ? sc01 : 0.055901699f);
            const v4u v = {pk_bf(c[0], c[1]), pk_bf(c[2], c[3]), pk_bf(c[4], c[5]), pk_bf(c[6], c[7])};
            st_ws(v, rs, lane * 16, OFF_RB2 + ((j * 4 + ks / 5) * 25 + (ks % 5) * 5 + m) * 1024);
        }
}

typedef v4u v4u_a2 __attribute__((aligned(2)));
__device__ __forceinline__ void wt_task(const Params &p, LAS unsigned char *L, __amdgpu_buffer_rsrc_t rs, int task, int ww, int lane) {
    const int w = task & 3, g = (task >> 2) & 3, part = task >> 4;
    const GAS uint16_t *src = (const GAS uint16_t *)(part ? p.wi : p.wr);
    const int tid = 64 * ww + lane;
    v4u t[20];
#pragma unroll
    for (int i = 0; i < 20; ++i) {
        const int idx = 256 * i + tid, row = (idx * 52429) >> 21, ch = idx - 40 * row, e = row >> 4;
        t[i] = *(const GAS v4u_a2 *)(src + (16 * w + (row & 15) + 64 * (16 * (e >> 2) + 4 * g + (e & 3))) * 321 + 1 + 8 * ch);
    }
    const int cl = lane & 15;
    v4u cm[3];
#pragma unroll
    for (int rd = 0; rd < 3; ++rd) {
        const int cg = 4 * (ww + 4 * rd) + (lane >> 4);
        cm[rd] = *(const GAS v4u *)((const GAS unsigned char *)&g_prep_tab + 43008 + (cg < 40 ? cg : 0) * 16);
    }
#pragma unroll
    for (int i = 0; i < 20; ++i) {
        const int idx = 256 * i + tid, row = (idx * 52429) >> 21, ch = idx - 40 * row;
        *(LAS v4u *)(L + row * 656 + ch * 16) = t[i];
    }
    __syncthreads();
#pragma unroll
    for (int rd = 0; rd < 3; ++rd) {
        if (rd < 2 || ww < 2) {
            const int cg = 4 * (ww + 4 * rd) + (lane >> 4);
            v4u v[8];
#pragma unroll
            for (int e = 0; e < 8; ++e) v[e] = lds_rd128(L, (16 * e + cl) * 656 + cg * 16);
#pragma unroll
            for (int cc = 0; cc < 8; ++cc) {
                const unsigned sel = (cc & 1) ? 0x07060302u : 0x05040100u;
                v4u o;
#pragma unroll
                for (int d = 0; d < 4; ++d) o[d] = __builtin_amdgcn_perm(v[2 * d + 1][cc >> 1], v[2 * d][cc >> 1], sel);
                const unsigned c = (cc & 1) ? cm[rd][cc >> 1] >> 16 : cm[rd][cc >> 1] & 0xFFFFu;
                st_ws(o, rs, c * 8192 + (16 * g + cl) * 16, OFF_WT + part * 4096 + w * 1024);
            }
        }
    }
}

__device__ __forceinline__ void w2t_task(const Params &p, LAS unsigned char *L, __amdgpu_buffer_rsrc_t rs, int task, int w, int lane) {
    v4u t[20];
    const GAS unsigned char *src = (const GAS unsigned char *)p.w2 + (64 * task + 16 * w) * 1280;
#pragma unroll
    for (int i = 0; i < 20; ++i) t[i] = *(const GAS v4u *)(src + (64 * i + lane) * 16);
#pragma unroll
    for (int i = 0; i < 20; ++i) {
        const int idx = 64 * i + lane, row = (idx * 52429) >> 22, ch = idx - 80 * row;
        *(LAS v4u *)(L + (16 * w + row) * 1312 + ch * 16) = t[i];
    }
    __syncthreads();
    const int g = lane >> 4, q4 = (lane >> 2) & 3, pi = lane & 3;
    const int a0 = (4 * g + q4) * 1312 + pi * 8;
#pragma unroll
    for (int fg = 0; fg < 2; ++fg)
#pragma unroll
        for (int i = 0; i < 10; ++i) {
            const int tau = 10 * w + i, j = tau / 5, m = tau - 5 * j;
            const int grp = 2 * task + fg, ww = grp / 12, ks = grp - 12 * ww;
            const v2u lo = lds_tr(L, a0 + fg * 32 * 1312 + tau * 32), hi = lds_tr(L, a0 + (fg * 32 + 16) * 1312 + tau * 32);
            st_ws(v4u{lo.x, lo.y, hi.x, hi.y}, rs, lane * 16, OFF_W2T + ((j * 4 + ww) * 60 + ks * 5 + m) * 1024);
        }
}

__global__ void __launch_bounds__(256, 2) k_prep(Params p) {
    extern __shared__ __attribute__((aligned(16))) unsigned char smem[];
    LAS unsigned char *L = (LAS unsigned char *)smem;
    const __amdgpu_buffer_rsrc_t rs = __builtin_amdgcn_make_buffer_rsrc((void *)p.ws, 0, (int)WS_USED, RSRC_W3);
    const int lane = threadIdx.x & 63, w = __builtin_amdgcn_readfirstlane(threadIdx.x >> 6);
    const int b = blockIdx.x;
    TSA(0);
    if (b < B_W1) dft_task<0>(p, L, rs, b, w, lane);
    else if (b < B_W2) dft_task<1>(p, L, rs, b - B_W1, w, lane);
    else if (b < B_B2) dft_task<2>(p, L, rs, b - B_W2, w, lane);
    else if (b < B_RB) dft_task<3>(p, L, rs, 0, w, lane);
    else if (b < B_WT) {
        const int k = b - B_RB;
        if (k < 16) st_ws(v4u{0u, 0u, 0u, 0u}, rs, threadIdx.x * 16, OFF_CTRL + k * 4096);
        else st_ws(v4u{0u, 0u, 0u, 0u}, rs, threadIdx.x * 16, OFF_SS + 16384 + (k - 16) * 4096);
        rb2_task(rs, k, w, lane);
    } else if (b < B_W2T) wt_task(p, L, rs, b - B_WT, w, lane);
    else w2t_task(p, L, rs, b - B_W2T, w, lane);
    TSA(7);
    TSA(8);
}

static void fill_params(Params &p, void *const *d_in, void *d_out, void *d_ws) {
    p.x = (const uint16_t *)d_in[0];
    p.wr = (const uint16_t *)d_in[1];
    p.wi = (const uint16_t *)d_in[2];
    p.w1 = (const uint16_t *)d_in[4];
    p.b1 = (const uint16_t *)d_in[5];
    p.w2 = (const uint16_t *)d_in[6];
    p.b2 = (const uint16_t *)d_in[7];
    p.out = (uint16_t *)d_out;
    p.ws = (unsigned char *)d_ws;
}
static void launch_prep(const Params &p, hipStream_t stream) {
    hipFuncSetAttribute((const void *)k_prep, hipFuncAttributeMaxDynamicSharedMemorySize, A_BYTES);
    k_prep<<<A_GRID, 256, A_BYTES, stream>>>(p);
}
template <int PB, int PE> static void launch_main(const Params &p, hipStream_t stream) {
    hipFuncSetAttribute((const void *)k_main<PB, PE>, hipFuncAttributeMaxDynamicSharedMemorySize, L_BYTES);
    k_main<PB, PE><<<256, 256, L_BYTES, stream>>>(p);
}

extern "C" void kernel_launch(void *const *d_in, const int *in_sizes, int n_in, void *d_out, int out_size, void *d_ws, size_t ws_size,
                              hipStream_t stream) {
    Params p;
    fill_params(p, d_in, d_out, d_ws);
    launch_prep(p, stream);
    launch_main<0, 4>(p, stream);
}
```

```cpp
#include <hip/hip_runtime.h>
#include <stdint.h>
#include <type_traits>

#define GAS __attribute__((address_space(1)))
#define LAS __attribute__((address_space(3)))
typedef unsigned v4u __attribute__((ext_vector_type(4)));
typedef unsigned v2u __attribute__((ext_vector_type(2)));
typedef float f32x4 __attribute__((ext_vector_type(4)));
typedef float f32x2 __attribute__((ext_vector_type(2)));
typedef short s16x4 __attribute__((ext_vector_type(4)));
typedef __bf16 bf16x2 __attribute__((ext_vector_type(2)));
typedef __bf16 bf16x8 __attribute__((ext_vector_type(8)));
typedef GAS unsigned gu32;
#define RLX_AGENT __ATOMIC_RELAXED, __HIP_MEMORY_SCOPE_AGENT

constexpr int S_ = 2048, D_ = 640, F_ = 1536;
constexpr int AUX_SC1 = 16;
constexpr int RSRC_W3 = 0x00020000;

#define XH_RS 4096
#define XH_BS 128
constexpr unsigned OFF_RS0 = 0;
constexpr unsigned OFF_B2P = 8192;
constexpr unsigned XH_BYTES = 32 * 82048;
constexpr unsigned OFF_XH0 = 16384;
constexpr unsigned XH0_RS = 32, XH0_TS = 640 * 32;
constexpr unsigned H_BYTES = S_ * F_ * 2;
constexpr unsigned OFF_PZ = OFF_XH0 + XH_BYTES;
constexpr unsigned OFF_SSP = OFF_PZ;
constexpr unsigned OFF_FLG = OFF_SSP + 32768;
constexpr unsigned OFF_XH1 = OFF_FLG + 4096;
constexpr unsigned OFF_PZ_END = OFF_XH1 + XH_BYTES;
constexpr unsigned OFF_XH2 = OFF_PZ_END;
constexpr unsigned OFF_H0 = OFF_XH2 + XH_BYTES;
constexpr unsigned OFF_XH3 = OFF_H0 + H_BYTES;
constexpr unsigned OFF_H1 = OFF_XH3 + XH_BYTES;
constexpr unsigned OFF_PM_END = OFF_H1 + H_BYTES;
constexpr unsigned WF_BYTES = D_ * F_ * 2;
constexpr unsigned OFF_W1F = OFF_PM_END;
constexpr unsigned OFF_W2F = OFF_W1F + WF_BYTES;
constexpr unsigned OFF_W2T = OFF_W2F + WF_BYTES;
constexpr unsigned OFF_RB2 = OFF_W2T + WF_BYTES;
constexpr unsigned OFF_WT = OFF_RB2 + D_ * D_ * 2;
#define PD_ST 64
#define RQ_ST 16
#define P2_ST 16
__host__ __device__ constexpr unsigned flag_off(unsigned t, unsigned st) { return (t >> 2) * st + (t & 3) * 4; }
constexpr unsigned OFF_REQ = OFF_WT + 320 * 8192;
constexpr unsigned OFF_ACK = OFF_REQ + 64 * RQ_ST;
constexpr unsigned OFF_PD = OFF_ACK + 64 * RQ_ST;
constexpr unsigned OFF_PD2 = OFF_PD + 512 * PD_ST;
constexpr unsigned WS_USED = OFF_PD2 + 256 * P2_ST;
constexpr unsigned EMPTY = 0xFFFFFFFFu;
constexpr unsigned SPIN_CAP = 1u << 16;

constexpr int L_TILE = 0;
constexpr int L_PART = 81920;
constexpr int L_MISC = L_PART + 61440;
constexpr int L_SS = L_MISC;
constexpr int L_SSB = L_MISC + 256;
constexpr int L_TS = L_MISC + 512;
constexpr int L_BYTES = L_MISC + 512 + 256;
constexpr unsigned OFF_TS = 200u << 20;
#define TS(n)
#define TS_DUMP()

template <int I, int N, class Fn> __device__ __forceinline__ void static_for(Fn &&f) {
    if constexpr (I < N) {
        f(std::integral_constant<int, I>{});
        static_for<I + 1, N>(f);
    }
}

__device__ __forceinline__ float u2f(unsigned u) { return __builtin_bit_cast(float, u); }
__device__ __forceinline__ float bf_lo(unsigned u) { return __builtin_bit_cast(float, u << 16); }
__device__ __forceinline__ float bf_hi(unsigned u) { return __builtin_bit_cast(float, u & 0xFFFF0000u); }
__device__ __forceinline__ unsigned pk_bf(float lo, float hi) {
    f32x2 v = {lo, hi};
    return __builtin_bit_cast(unsigned, __builtin_convertvector(v, bf16x2));
}
__host__ __device__ __forceinline__ int pos2tok(int p) {
    const int l = p >> 3, t = (p >> 2) & 1, r = p & 3;
    const int w = l >> 6, g = (l >> 4) & 3, bl = l & 15;
    return 32 * (16 * w + 4 * g + r) + 16 * t + bl;
}

#define MFMA_OP "v_mfma_f32_16x16x32_bf16 "
template <int Q> __device__ __forceinline__ void mf_ra0(f32x4 &c, v4u b) {
    asm volatile(MFMA_OP "%0, a[%c2:%c3], %1, 0" : "=&v"(c) : "v"(b), "i"(4 * Q), "i"(4 * Q + 3));
}
template <int Q> __device__ __forceinline__ void mf_ra(f32x4 &c, v4u b) {
    asm volatile(MFMA_OP "%0, a[%c2:%c3], %1, %0" : "+v"(c) : "v"(b), "i"(4 * Q), "i"(4 * Q + 3));
}
template <int Q> __device__ __forceinline__ void mf_rb0(f32x4 &c, v4u a) {
    asm volatile(MFMA_OP "%0, %1, a[%c2:%c3], 0" : "=&v"(c) : "v"(a), "i"(4 * Q), "i"(4 * Q + 3));
}
template <int Q> __device__ __forceinline__ void mf_rb(f32x4 &c, v4u a) {
    asm volatile(MFMA_OP "%0, %1, a[%c2:%c3], %0" : "+v"(c) : "v"(a), "i"(4 * Q), "i"(4 * Q + 3));
}
__device__ __forceinline__ void mf_vv0(f32x4 &c, v4u a, v4u b) {
    asm volatile(MFMA_OP "%0, %1, %2, 0" : "=&v"(c) : "v"(a), "v"(b));
}
__device__ __forceinline__ void mf_vv(f32x4 &c, v4u a, v4u b) {
    asm volatile(MFMA_OP "%0, %1, %2, %0" : "+v"(c) : "v"(a), "v"(b));
}
__device__ __forceinline__ void mf_cplx2(f32x4 &c, v4u a0, v4u b0, v4u a1, v4u b1) {
    asm volatile("s_nop 1\n\t" MFMA_OP "%0, %1, %2, 0\n\t" MFMA_OP "%0, %3, %4, %0"
                 : "=&v"(c) : "v"(a0), "v"(b0), "v"(a1), "v"(b1));
}
__device__ __forceinline__ void mf_cplx4(f32x4 &c, v4u a0, v4u b0, v4u a1, v4u b1, v4u a2, v4u b2, v4u a3, v4u b3) {
    asm volatile("s_nop 1\n\t" MFMA_OP "%0, %1, %2, 0\n\t" MFMA_OP "%0, %3, %4, %0\n\t" MFMA_OP "%0, %5, %6, %0\n\t" MFMA_OP "%0, %7, %8, %0"
                 : "=&v"(c) : "v"(a0), "v"(b0), "v"(a1), "v"(b1), "v"(a2), "v"(b2), "v"(a3), "v"(b3));
}
#define MF_FENCE4(a, b, c, d) asm volatile("s_nop 7" : "+v"(a), "+v"(b), "+v"(c), "+v"(d))
#define MF_FENCE0(a, b, c, d) asm volatile("" : "+v"(a), "+v"(b), "+v"(c), "+v"(d))

template <int Q, int IMM> __device__ __forceinline__ void ring_ld(const GAS unsigned char *sbase, unsigned voff) {
    asm volatile("global_load_dwordx4 a[%c2:%c3], %0, %1 offset:%c4 sc1" ::"v"(voff), "s"(sbase), "i"(4 * Q), "i"(4 * Q + 3), "i"(IMM));
}
template <int Q> __device__ __forceinline__ void ring_ld_q(const GAS unsigned char *run, unsigned voff) {
    ring_ld<Q, (Q & 3) * 1024>(run + (Q >> 2) * 4096, voff);
}
template <int LO, int HI> __device__ __forceinline__ void ring_fill(const GAS unsigned char *run, unsigned voff) {
    static_for<LO, HI>([&](auto q) { ring_ld_q<q.value>(run, voff); });
}
#define DRAIN() asm volatile("s_waitcnt vmcnt(0)" ::: "memory")

__device__ __forceinline__ v4u ld_sc1(__amdgpu_buffer_rsrc_t rs, int voff, int soff) {
    return __builtin_amdgcn_raw_buffer_load_b128(rs, voff, soff, AUX_SC1);
}
__device__ __forceinline__ v4u ld_pl(__amdgpu_buffer_rsrc_t rs, int voff, int soff) {
    return __builtin_amdgcn_raw_buffer_load_b128(rs, voff, soff, 0);
}
__device__ __forceinline__ void st_sc1(v4u v, __amdgpu_buffer_rsrc_t rs, int voff, int soff) {
    __builtin_amdgcn_raw_buffer_store_b128(v, rs, voff + soff, 0, AUX_SC1);
}
__device__ __forceinline__ v4u lds_rd128(LAS unsigned char *L, int off) { return *(LAS v4u *)(L + off); }
__device__ __forceinline__ v2u lds_tr(LAS unsigned char *L, int off) {
    return __builtin_bit_cast(v2u, __builtin_amdgcn_ds_read_tr16_b64_v4i16((LAS s16x4 *)(L + off)));
}

struct Params {
    const uint16_t *x, *wr, *wi, *w1, *b1, *w2, *b2;
    uint16_t *out;
    unsigned char *ws;
};

__device__ __forceinline__ v4u not_empty(v4u v) {
    return v4u{min(v.x, EMPTY - 1u), min(v.y, EMPTY - 1u), min(v.z, EMPTY - 1u), min(v.w, EMPTY - 1u)};
}
#define ASM_STAGE \
    "s_add_u32 s60, %[xin], 0\n" \
    "buffer_load_dwordx4 v[128:131], %[voff], %[rs], s60 offen sc1\n" \
    "s_add_u32 s60, %[xin], 131072\n" \
    "buffer_load_dwordx4 v[132:135], %[voff], %[rs], s60 offen sc1\n" \
    "s_add_u32 s60, %[xin], 262144\n" \
    "buffer_load_dwordx4 v[136:139], %[voff], %[rs], s60 offen sc1\n" \
    "s_add_u32 s60, %[xin], 393216\n" \
    "buffer_load_dwordx4 v[140:143], %[voff], %[rs], s60 offen sc1\n" \
    "s_add_u32 s60, %[xin], 524288\n" \
    "buffer_load_dwordx4 v[144:147], %[voff], %[rs], s60 offen sc1\n" \
    "s_add_u32 s60, %[xin], 655360\n" \
    "buffer_load_dwordx4 v[148:151], %[voff], %[rs], s60 offen sc1\n" \
    "s_add_u32 s60, %[xin], 786432\n" \
    "buffer_load_dwordx4 v[152:155], %[voff], %[rs], s60 offen sc1\n" \
    "s_add_u32 s60, %[xin], 917504\n" \
    "buffer_load_dwordx4 v[156:159], %[voff], %[rs], s60 offen sc1\n" \
    "s_add_u32 s60, %[xin], 1048576\n" \
    "buffer_load_dwordx4 v[160:163], %[voff], %[rs], s60 offen sc1\n" \
    "s_add_u32 s60, %[xin], 1179648\n" \
    "buffer_load_dwordx4 v[164:167], %[voff], %[rs], s60 offen sc1\n" \
    "s_add_u32 s60, %[xin], 1310720\n" \
    "buffer_load_dwordx4 v[168:171], %[voff], %[rs], s60 offen sc1\n" \
    "s_add_u32 s60, %[xin], 1441792\n" \
    "buffer_load_dwordx4 v[172:175], %[voff], %[rs], s60 offen sc1\n" \
    "s_add_u32 s60, %[xin], 1572864\n" \
    "buffer_load_dwordx4 v[176:179], %[voff], %[rs], s60 offen sc1\n" \
    "s_add_u32 s60, %[xin], 1703936\n" \
    "buffer_load_dwordx4 v[180:183], %[voff], %[rs], s60 offen sc1\n" \
    "s_add_u32 s60, %[xin], 1835008\n" \
    "buffer_load_dwordx4 v[184:187], %[voff], %[rs], s60 offen sc1\n" \
    "s_add_u32 s60, %[xin], 1966080\n" \
    "buffer_load_dwordx4 v[188:191], %[voff], %[rs], s60 offen sc1\n" \
    "s_add_u32 s60, %[xin], 2097152\n" \
    "buffer_load_dwordx4 v[192:195], %[voff], %[rs], s60 offen sc1\n" \
    "s_add_u32 s60, %[xin], 2228224\n" \
    "buffer_load_dwordx4 v[196:199], %[voff], %[rs], s60 offen sc1\n" \
    "s_add_u32 s60, %[xin], 2359296\n" \
    "buffer_load_dwordx4 v[200:203], %[voff], %[rs], s60 offen sc1\n" \
    "s_add_u32 s60, %[xin], 2490368\n" \
    "buffer_load_dwordx4 v[204:207], %[voff], %[rs], s60 offen sc1\n" \
    "s_mov_b32 s61, 15\n" \
    "s_mov_b32 s62, 65536\n" \
    "s_waitcnt vmcnt(15)\n" \
    "v_max3_u32 v208, v128, v129, v130\n" \
    "v_max3_u32 v208, v208, v131, v132\n" \
    "v_max3_u32 v208, v208, v133, v134\n" \
    "v_max3_u32 v208, v208, v135, v136\n" \
    "v_max3_u32 v208, v208, v137, v138\n" \
    "v_max3_u32 v208, v208, v139, v140\n" \
    "v_max3_u32 v208, v208, v141, v142\n" \
    "v_max3_u32 v208, v208, v143, v144\n" \
    "v_max3_u32 v208, v208, v145, v146\n" \
    "v_max_u32 v208, v208, v147\n" \
    "v_cmp_eq_u32 vcc, -1, v208\n" \
    "s_cbranch_vccnz 101f\n" \
    "ds_write_b128 %[la], v[128:131] offset:0\n" \
    "ds_write_b128 %[la], v[132:135] offset:4096\n" \
    "ds_write_b128 %[la], v[136:139] offset:8192\n" \
    "ds_write_b128 %[la], v[140:143] offset:12288\n" \
    "ds_write_b128 %[la], v[144:147] offset:16384\n" \
    "s_bitset0_b32 s61, 0\n" \
    "s_branch 102f\n" \
    "101:\n" \
    "s_mov_b64 exec, vcc\n" \
    "s_add_u32 s60, %[xin], 0\n" \
    "buffer_load_dwordx4 v[128:131], %[voff], %[rs], s60 offen sc1\n" \
    "s_add_u32 s60, %[xin], 131072\n" \
    "buffer_load_dwordx4 v[132:135], %[voff], %[rs], s60 offen sc1\n" \
    "s_add_u32 s60, %[xin], 262144\n" \
    "buffer_load_dwordx4 v[136:139], %[voff], %[rs], s60 offen sc1\n" \
    "s_add_u32 s60, %[xin], 393216\n" \
    "buffer_load_dwordx4 v[140:143], %[voff], %[rs], s60 offen sc1\n" \
    "s_add_u32 s60, %[xin], 524288\n" \
    "buffer_load_dwordx4 v[144:147], %[voff], %[rs], s60 offen sc1\n" \
    "s_mov_b64 exec, -1\n" \
    "102:\n" \
    "s_waitcnt vmcnt(10)\n" \
    "v_max3_u32 v208, v148, v149, v150\n" \
    "v_max3_u32 v208, v208, v151, v152\n" \
    "v_max3_u32 v208, v208, v153, v154\n" \
    "v_max3_u32 v208, v208, v155, v156\n" \
    "v_max3_u32 v208, v208, v157, v158\n" \
    "v_max3_u32 v208, v208, v159, v160\n" \
    "v_max3_u32 v208, v208, v161, v162\n" \
    "v_max3_u32 v208, v208, v163, v164\n" \
    "v_max3_u32 v208, v208, v165, v166\n" \
    "v_max_u32 v208, v208, v167\n" \
    "v_cmp_eq_u32 vcc, -1, v208\n" \
    "s_cbranch_vccnz 111f\n" \
    "ds_write_b128 %[la], v[148:151] offset:20480\n" \
    "ds_write_b128 %[la], v[152:155] offset:24576\n" \
    "ds_write_b128 %[la], v[156:159] offset:28672\n" \
    "ds_write_b128 %[la], v[160:163] offset:32768\n" \
    "ds_write_b128 %[la], v[164:167] offset:36864\n" \
    "s_bitset0_b32 s61, 1\n" \
    "s_branch 112f\n" \
    "111:\n" \
    "s_mov_b64 exec, vcc\n" \
    "s_add_u32 s60, %[xin], 655360\n" \
    "buffer_load_dwordx4 v[148:151], %[voff], %[rs], s60 offen sc1\n" \
    "s_add_u32 s60, %[xin], 786432\n" \
    "buffer_load_dwordx4 v[152:155], %[voff], %[rs], s60 offen sc1\n" \
    "s_add_u32 s60, %[xin], 917504\n" \
    "buffer_load_dwordx4 v[156:159], %[voff], %[rs], s60 offen sc1\n" \
    "s_add_u32 s60, %[xin], 1048576\n" \
    "buffer_load_dwordx4 v[160:163], %[voff], %[rs], s60 offen sc1\n" \
    "s_add_u32 s60, %[xin], 1179648\n" \
    "buffer_load_dwordx4 v[164:167], %[voff], %[rs], s60 offen sc1\n" \
    "s_mov_b64 exec, -1\n" \
    "112:\n" \
    "s_waitcnt vmcnt(5)\n" \
    "v_max3_u32 v208, v168, v169, v170\n" \
    "v_max3_u32 v208, v208, v171, v172\n" \
    "v_max3_u32 v208, v208, v173, v174\n" \
    "v_max3_u32 v208, v208, v175, v176\n" \
    "v_max3_u32 v208, v208, v177, v178\n" \
    "v_max3_u32 v208, v208, v179, v180\n" \
    "v_max3_u32 v208, v208, v181, v182\n" \
    "v_max3_u32 v208, v208, v183, v184\n" \
    "v_max3_u32 v208, v208, v185, v186\n" \
    "v_max_u32 v208, v208, v187\n" \
    "v_cmp_eq_u32 vcc, -1, v208\n" \
    "s_cbranch_vccnz 121f\n" \
    "ds_write_b128 %[la], v[168:171] offset:40960\n" \
    "ds_write_b128 %[la], v[172:175] offset:45056\n" \
    "ds_write_b128 %[la], v[176:179] offset:49152\n" \
    "ds_write_b128 %[la], v[180:183] offset:53248\n" \
    "ds_write_b128 %[la], v[184:187] offset:57344\n" \
    "s_bitset0_b32 s61, 2\n" \
    "s_branch 122f\n" \
    "121:\n" \
    "s_mov_b64 exec, vcc\n" \
    "s_add_u32 s60, %[xin], 1310720\n" \
    "buffer_load_dwordx4 v[168:171], %[voff], %[rs], s60 offen sc1\n" \
    "s_add_u32 s60, %[xin], 1441792\n" \
    "buffer_load_dwordx4 v[172:175], %[voff], %[rs], s60 offen sc1\n" \
    "s_add_u32 s60, %[xin], 1572864\n" \
    "buffer_load_dwordx4 v[176:179], %[voff], %[rs], s60 offen sc1\n" \
    "s_add_u32 s60, %[xin], 1703936\n" \
    "buffer_load_dwordx4 v[180:183], %[voff], %[rs], s60 offen sc1\n" \
    "s_add_u32 s60, %[xin], 1835008\n" \
    "buffer_load_dwordx4 v[184:187], %[voff], %[rs], s60 offen sc1\n" \
    "s_mov_b64 exec, -1\n" \
    "122:\n" \
    "s_waitcnt vmcnt(0)\n" \
    "v_max3_u32 v208, v188, v189, v190\n" \
    "v_max3_u32 v208, v208, v191, v192\n" \
    "v_max3_u32 v208, v208, v193, v194\n" \
    "v_max3_u32 v208, v208, v195, v196\n" \
    "v_max3_u32 v208, v208, v197, v198\n" \
    "v_max3_u32 v208, v208, v199, v200\n" \
    "v_max3_u32 v208, v208, v201, v202\n" \
    "v_max3_u32 v208, v208, v203, v204\n" \
    "v_max3_u32 v208, v208, v205, v206\n" \
    "v_max_u32 v208, v208, v207\n" \
    "v_cmp_eq_u32 vcc, -1, v208\n" \
    "s_cbranch_vccnz 131f\n" \
    "ds_write_b128 %[la], v[188:191] offset:61440\n" \
    "ds_write_b128 %[lb], v[192:195] offset:0\n" \
    "ds_write_b128 %[lb], v[196:199] offset:4096\n" \
    "ds_write_b128 %[lb], v[200:203] offset:8192\n" \
    "ds_write_b128 %[lb], v[204:207] offset:12288\n" \
    "s_bitset0_b32 s61, 3\n" \
    "s_branch 132f\n" \
    "131:\n" \
    "s_mov_b64 exec, vcc\n" \
    "s_add_u32 s60, %[xin], 1966080\n" \
    "buffer_load_dwordx4 v[188:191], %[voff], %[rs], s60 offen sc1\n" \
    "s_add_u32 s60, %[xin], 2097152\n" \
    "buffer_load_dwordx4 v[192:195], %[voff], %[rs], s60 offen sc1\n" \
    "s_add_u32 s60, %[xin], 2228224\n" \
    "buffer_load_dwordx4 v[196:199], %[voff], %[rs], s60 offen sc1\n" \
    "s_add_u32 s60, %[xin], 2359296\n" \
    "buffer_load_dwordx4 v[200:203], %[voff], %[rs], s60 offen sc1\n" \
    "s_add_u32 s60, %[xin], 2490368\n" \
    "buffer_load_dwordx4 v[204:207], %[voff], %[rs], s60 offen sc1\n" \
    "s_mov_b64 exec, -1\n" \
    "132:\n" \
    "90:\n" \
    "s_cmp_eq_u32 s61, 0\n" \
    "s_cbranch_scc1 99f\n" \
    "s_sub_u32 s62, s62, 1\n" \
    "s_cbranch_scc1 99f\n" \
    "s_waitcnt vmcnt(0)\n" \
    "s_bitcmp1_b32 s61, 0\n" \
    "s_cbranch_scc0 202f\n" \
    "v_max3_u32 v208, v128, v129, v130\n" \
    "v_max3_u32 v208, v208, v131, v132\n" \
    "v_max3_u32 v208, v208, v133, v134\n" \
    "v_max3_u32 v208, v208, v135, v136\n" \
    "v_max3_u32 v208, v208, v137, v138\n" \
    "v_max3_u32 v208, v208, v139, v140\n" \
    "v_max3_u32 v208, v208, v141, v142\n" \
    "v_max3_u32 v208, v208, v143, v144\n" \
    "v_max3_u32 v208, v208, v145, v146\n" \
    "v_max_u32 v208, v208, v147\n" \
    "v_cmp_eq_u32 vcc, -1, v208\n" \
    "s_cbranch_vccnz 201f\n" \
    "ds_write_b128 %[la], v[128:131] offset:0\n" \
    "ds_write_b128 %[la], v[132:135] offset:4096\n" \
    "ds_write_b128 %[la], v[136:139] offset:8192\n" \
    "ds_write_b128 %[la], v[140:143] offset:12288\n" \
    "ds_write_b128 %[la], v[144:147] offset:16384\n" \
    "s_bitset0_b32 s61, 0\n" \
    "s_branch 202f\n" \
    "201:\n" \
    "s_mov_b64 exec, vcc\n" \
    "s_add_u32 s60, %[xin], 0\n" \
    "buffer_load_dwordx4 v[128:131], %[voff], %[rs], s60 offen sc1\n" \
    "s_add_u32 s60, %[xin], 131072\n" \
    "buffer_load_dwordx4 v[132:135], %[voff], %[rs], s60 offen sc1\n" \
    "s_add_u32 s60, %[xin], 262144\n" \
    "buffer_load_dwordx4 v[136:139], %[voff], %[rs], s60 offen sc1\n" \
    "s_add_u32 s60, %[xin], 393216\n" \
    "buffer_load_dwordx4 v[140:143], %[voff], %[rs], s60 offen sc1\n" \
    "s_add_u32 s60, %[xin], 524288\n" \
    "buffer_load_dwordx4 v[144:147], %[voff], %[rs], s60 offen sc1\n" \
    "s_mov_b64 exec, -1\n" \
    "202:\n" \
    "s_bitcmp1_b32 s61, 1\n" \
    "s_cbranch_scc0 212f\n" \
    "v_max3_u32 v208, v148, v149, v150\n" \
    "v_max3_u32 v208, v208, v151, v152\n" \
    "v_max3_u32 v208, v208, v153, v154\n" \
    "v_max3_u32 v208, v208, v155, v156\n" \
    "v_max3_u32 v208, v208, v157, v158\n" \
    "v_max3_u32 v208, v208, v159, v160\n" \
    "v_max3_u32 v208, v208, v161, v162\n" \
    "v_max3_u32 v208, v208, v163, v164\n" \
    "v_max3_u32 v208, v208, v165, v166\n" \
    "v_max_u32 v208, v208, v167\n" \
    "v_cmp_eq_u32 vcc, -1, v208\n" \
    "s_cbranch_vccnz 211f\n" \
    "ds_write_b128 %[la], v[148:151] offset:20480\n" \
    "ds_write_b128 %[la], v[152:155] offset:24576\n" \
    "ds_write_b128 %[la], v[156:159] offset:28672\n" \
    "ds_write_b128 %[la], v[160:163] offset:32768\n" \
    "ds_write_b128 %[la], v[164:167] offset:36864\n" \
    "s_bitset0_b32 s61, 1\n" \
    "s_branch 212f\n" \
    "211:\n" \
    "s_mov_b64 exec, vcc\n" \
    "s_add_u32 s60, %[xin], 655360\n" \
    "buffer_load_dwordx4 v[148:151], %[voff], %[rs], s60 offen sc1\n" \
    "s_add_u32 s60, %[xin], 786432\n" \
    "buffer_load_dwordx4 v[152:155], %[voff], %[rs], s60 offen sc1\n" \
    "s_add_u32 s60, %[xin], 917504\n" \
    "buffer_load_dwordx4 v[156:159], %[voff], %[rs], s60 offen sc1\n" \
    "s_add_u32 s60, %[xin], 1048576\n" \
    "buffer_load_dwordx4 v[160:163], %[voff], %[rs], s60 offen sc1\n" \
    "s_add_u32 s60, %[xin], 1179648\n" \
    "buffer_load_dwordx4 v[164:167], %[voff], %[rs], s60 offen sc1\n" \
    "s_mov_b64 exec, -1\n" \
    "212:\n" \
    "s_bitcmp1_b32 s61, 2\n" \
    "s_cbranch_scc0 222f\n" \
    "v_max3_u32 v208, v168, v169, v170\n" \
    "v_max3_u32 v208, v208, v171, v172\n" \
    "v_max3_u32 v208, v208, v173, v174\n" \
    "v_max3_u32 v208, v208, v175, v176\n" \
    "v_max3_u32 v208, v208, v177, v178\n" \
    "v_max3_u32 v208, v208, v179, v180\n" \
    "v_max3_u32 v208, v208, v181, v182\n" \
    "v_max3_u32 v208, v208, v183, v184\n" \
    "v_max3_u32 v208, v208, v185, v186\n" \
    "v_max_u32 v208, v208, v187\n" \
    "v_cmp_eq_u32 vcc, -1, v208\n" \
    "s_cbranch_vccnz 221f\n" \
    "ds_write_b128 %[la], v[168:171] offset:40960\n" \
    "ds_write_b128 %[la], v[172:175] offset:45056\n" \
    "ds_write_b128 %[la], v[176:179] offset:49152\n" \
    "ds_write_b128 %[la], v[180:183] offset:53248\n" \
    "ds_write_b128 %[la], v[184:187] offset:57344\n" \
    "s_bitset0_b32 s61, 2\n" \
    "s_branch 222f\n" \
    "221:\n" \
    "s_mov_b64 exec, vcc\n" \
    "s_add_u32 s60, %[xin], 1310720\n" \
    "buffer_load_dwordx4 v[168:171], %[voff], %[rs], s60 offen sc1\n" \
    "s_add_u32 s60, %[xin], 1441792\n" \
    "buffer_load_dwordx4 v[172:175], %[voff], %[rs], s60 offen sc1\n" \
    "s_add_u32 s60, %[xin], 1572864\n" \
    "buffer_load_dwordx4 v[176:179], %[voff], %[rs], s60 offen sc1\n" \
    "s_add_u32 s60, %[xin], 1703936\n" \
    "buffer_load_dwordx4 v[180:183], %[voff], %[rs], s60 offen sc1\n" \
    "s_add_u32 s60, %[xin], 1835008\n" \
    "buffer_load_dwordx4 v[184:187], %[voff], %[rs], s60 offen sc1\n" \
    "s_mov_b64 exec, -1\n" \
    "222:\n" \
    "s_bitcmp1_b32 s61, 3\n" \
    "s_cbranch_scc0 232f\n" \
    "v_max3_u32 v208, v188, v189, v190\n" \
    "v_max3_u32 v208, v208, v191, v192\n" \
    "v_max3_u32 v208, v208, v193, v194\n" \
    "v_max3_u32 v208, v208, v195, v196\n" \
    "v_max3_u32 v208, v208, v197, v198\n" \
    "v_max3_u32 v208, v208, v199, v200\n" \
    "v_max3_u32 v208, v208, v201, v202\n" \
    "v_max3_u32 v208, v208, v203, v204\n" \
    "v_max3_u32 v208, v208, v205, v206\n" \
    "v_max_u32 v208, v208, v207\n" \
    "v_cmp_eq_u32 vcc, -1, v208\n" \
    "s_cbranch_vccnz 231f\n" \
    "ds_write_b128 %[la], v[188:191] offset:61440\n" \
    "ds_write_b128 %[lb], v[192:195] offset:0\n" \
    "ds_write_b128 %[lb], v[196:199] offset:4096\n" \
    "ds_write_b128 %[lb], v[200:203] offset:8192\n" \
    "ds_write_b128 %[lb], v[204:207] offset:12288\n" \
    "s_bitset0_b32 s61, 3\n" \
    "s_branch 232f\n" \
    "231:\n" \
    "s_mov_b64 exec, vcc\n" \
    "s_add_u32 s60, %[xin], 1966080\n" \
    "buffer_load_dwordx4 v[188:191], %[voff], %[rs], s60 offen sc1\n" \
    "s_add_u32 s60, %[xin], 2097152\n" \
    "buffer_load_dwordx4 v[192:195], %[voff], %[rs], s60 offen sc1\n" \
    "s_add_u32 s60, %[xin], 2228224\n" \
    "buffer_load_dwordx4 v[196:199], %[voff], %[rs], s60 offen sc1\n" \
    "s_add_u32 s60, %[xin], 2359296\n" \
    "buffer_load_dwordx4 v[200:203], %[voff], %[rs], s60 offen sc1\n" \
    "s_add_u32 s60, %[xin], 2490368\n" \
    "buffer_load_dwordx4 v[204:207], %[voff], %[rs], s60 offen sc1\n" \
    "s_mov_b64 exec, -1\n" \
    "232:\n" \
    "s_branch 90b\n" \
    "99:\n" \
    "s_waitcnt vmcnt(0) lgkmcnt(0)\n"
#define ASM_STAGE_CLOB "v128", "v129", "v130", "v131", "v132", "v133", "v134", "v135", "v136", "v137", "v138", "v139", "v140", "v141", "v142", "v143", "v144", "v145", "v146", "v147", "v148", "v149", "v150", "v151", "v152", "v153", "v154", "v155", "v156", "v157", "v158", "v159", "v160", "v161", "v162", "v163", "v164", "v165", "v166", "v167", "v168", "v169", "v170", "v171", "v172", "v173", "v174", "v175", "v176", "v177", "v178", "v179", "v180", "v181", "v182", "v183", "v184", "v185", "v186", "v187", "v188", "v189", "v190", "v191", "v192", "v193", "v194", "v195", "v196", "v197", "v198", "v199", "v200", "v201", "v202", "v203", "v204", "v205", "v206", "v207", "v208", "s60", "s61", "s62", "vcc", "scc", "memory"
#define ASM_CIN_ISSUE \
    "s_add_u32 s60, %[xrow], 0\n" \
    "buffer_load_dwordx4 v[128:131], %[vx], %[rs], s60 offen sc1\n" \
    "s_add_u32 s60, %[xrow], 4096\n" \
    "buffer_load_dwordx4 v[132:135], %[vx], %[rs], s60 offen sc1\n" \
    "s_add_u32 s60, %[xrow], 8192\n" \
    "buffer_load_dwordx4 v[136:139], %[vx], %[rs], s60 offen sc1\n" \
    "s_add_u32 s60, %[xrow], 12288\n" \
    "buffer_load_dwordx4 v[140:143], %[vx], %[rs], s60 offen sc1\n" \
    "s_add_u32 s60, %[ssp], 0\n" \
    "buffer_load_dwordx4 v[144:147], %[vw], %[rs], s60 offen sc1\n" \
    "s_add_u32 s60, %[ssp], 4096\n" \
    "buffer_load_dwordx4 v[148:151], %[vw], %[rs], s60 offen sc1\n" \
    "s_add_u32 s60, %[ssp], 8192\n" \
    "buffer_load_dwordx4 v[152:155], %[vw], %[rs], s60 offen sc1\n" \
    "s_add_u32 s60, %[ssp], 12288\n" \
    "buffer_load_dwordx4 v[156:159], %[vw], %[rs], s60 offen sc1\n" \
    "s_add_u32 s60, %[ssp], 16384\n" \
    "buffer_load_dwordx4 v[160:163], %[vw], %[rs], s60 offen sc1\n" \
    "s_add_u32 s60, %[ssp], 20480\n" \
    "buffer_load_dwordx4 v[164:167], %[vw], %[rs], s60 offen sc1\n" \
    "s_add_u32 s60, %[ssp], 24576\n" \
    "buffer_load_dwordx4 v[168:171], %[vw], %[rs], s60 offen sc1\n" \
    "s_add_u32 s60, %[ssp], 28672\n" \
    "buffer_load_dwordx4 v[172:175], %[vw], %[rs], s60 offen sc1\n"
#define ASM_CIN_WAIT \
    "s_mov_b32 s62, 65536\n" \
    "90:\n" \
    "s_waitcnt vmcnt(0)\n" \
    "s_mov_b32 s61, 0\n" \
    "v_max3_u32 v176, v128, v129, v130\n" \
    "v_max3_u32 v176, v176, v131, v132\n" \
    "v_max3_u32 v176, v176, v133, v134\n" \
    "v_max3_u32 v176, v176, v135, v136\n" \
    "v_max3_u32 v176, v176, v137, v138\n" \
    "v_max3_u32 v176, v176, v139, v140\n" \
    "v_max3_u32 v176, v176, v141, v142\n" \
    "v_max_u32 v176, v176, v143\n" \
    "v_cmp_eq_u32 vcc, -1, v176\n" \
    "s_cbranch_vccz 10f\n" \
    "s_mov_b64 exec, vcc\n" \
    "s_add_u32 s60, %[xrow], 0\n" \
    "buffer_load_dwordx4 v[128:131], %[vx], %[rs], s60 offen sc1\n" \
    "s_add_u32 s60, %[xrow], 4096\n" \
    "buffer_load_dwordx4 v[132:135], %[vx], %[rs], s60 offen sc1\n" \
    "s_add_u32 s60, %[xrow], 8192\n" \
    "buffer_load_dwordx4 v[136:139], %[vx], %[rs], s60 offen sc1\n" \
    "s_add_u32 s60, %[xrow], 12288\n" \
    "buffer_load_dwordx4 v[140:143], %[vx], %[rs], s60 offen sc1\n" \
    "s_mov_b64 exec, -1\n" \
    "s_mov_b32 s61, 1\n" \
    "10:\n" \
    "v_max3_u32 v176, v144, v145, v146\n" \
    "v_max3_u32 v176, v176, v147, v148\n" \
    "v_max3_u32 v176, v176, v149, v150\n" \
    "v_max3_u32 v176, v176, v151, v152\n" \
    "v_max3_u32 v176, v176, v153, v154\n" \
    "v_max3_u32 v176, v176, v155, v156\n" \
    "v_max3_u32 v176, v176, v157, v158\n" \
    "v_max_u32 v176, v176, v159\n" \
    "v_cmp_eq_u32 vcc, -1, v176\n" \
    "s_cbranch_vccz 11f\n" \
    "s_mov_b64 exec, vcc\n" \
    "s_add_u32 s60, %[ssp], 0\n" \
    "buffer_load_dwordx4 v[144:147], %[vw], %[rs], s60 offen sc1\n" \
    "s_add_u32 s60, %[ssp], 4096\n" \
    "buffer_load_dwordx4 v[148:151], %[vw], %[rs], s60 offen sc1\n" \
    "s_add_u32 s60, %[ssp], 8192\n" \
    "buffer_load_dwordx4 v[152:155], %[vw], %[rs], s60 offen sc1\n" \
    "s_add_u32 s60, %[ssp], 12288\n" \
    "buffer_load_dwordx4 v[156:159], %[vw], %[rs], s60 offen sc1\n" \
    "s_mov_b64 exec, -1\n" \
    "s_mov_b32 s61, 1\n" \
    "11:\n" \
    "v_max3_u32 v176, v160, v161, v162\n" \
    "v_max3_u32 v176, v176, v163, v164\n" \
    "v_max3_u32 v176, v176, v165, v166\n" \
    "v_max3_u32 v176, v176, v167, v168\n" \
    "v_max3_u32 v176, v176, v169, v170\n" \
    "v_max3_u32 v176, v176, v171, v172\n" \
    "v_max3_u32 v176, v176, v173, v174\n" \
    "v_max_u32 v176, v176, v175\n" \
    "v_cmp_eq_u32 vcc, -1, v176\n" \
    "s_cbranch_vccz 12f\n" \
    "s_mov_b64 exec, vcc\n" \
    "s_add_u32 s60, %[ssp], 16384\n" \
    "buffer_load_dwordx4 v[160:163], %[vw], %[rs], s60 offen sc1\n" \
    "s_add_u32 s60, %[ssp], 20480\n" \
    "buffer_load_dwordx4 v[164:167], %[vw], %[rs], s60 offen sc1\n" \
    "s_add_u32 s60, %[ssp], 24576\n" \
    "buffer_load_dwordx4 v[168:171], %[vw], %[rs], s60 offen sc1\n" \
    "s_add_u32 s60, %[ssp], 28672\n" \
    "buffer_load_dwordx4 v[172:175], %[vw], %[rs], s60 offen sc1\n" \
    "s_mov_b64 exec, -1\n" \
    "s_mov_b32 s61, 1\n" \
    "12:\n" \
    "s_cmp_eq_u32 s61, 0\n" \
    "s_cbranch_scc1 99f\n" \
    "s_sub_u32 s62, s62, 1\n" \
    "s_cbranch_scc0 90b\n" \
    "99:\n" \
    "s_waitcnt vmcnt(0)\n"
#define ASM_CIN_CLOB "v176", "s60", "s61", "s62", "vcc", "scc", "memory"
#define ASM_G2_MID \
    "v_mov_b32 v228, -1\n" \
    "v_mov_b32 v229, -1\n" \
    "v_mov_b32 v230, -1\n" \
    "v_mov_b32 v231, -1\n" \
    "s_add_u32 s60, %[hoff], 0\n" \
    "buffer_load_dwordx4 v[128:131], %[voa], %[rs], s60 offen sc1\n" \
    "buffer_load_dwordx4 v[132:135], %[voa], %[rs], s60 offen offset:1024 sc1\n" \
    "buffer_load_dwordx4 v[136:139], %[vob], %[rs], s60 offen sc1\n" \
    "buffer_load_dwordx4 v[140:143], %[vob], %[rs], s60 offen offset:1024 sc1\n" \
    "s_add_u32 s61, %[hoff], 4096\n" \
    "buffer_load_dwordx4 v[144:147], %[voa], %[rs], s61 offen sc1\n" \
    "buffer_load_dwordx4 v[148:151], %[voa], %[rs], s61 offen offset:1024 sc1\n" \
    "buffer_load_dwordx4 v[152:155], %[vob], %[rs], s61 offen sc1\n" \
    "buffer_load_dwordx4 v[156:159], %[vob], %[rs], s61 offen offset:1024 sc1\n" \
    "s_add_u32 s62, %[hoff], 8192\n" \
    "buffer_load_dwordx4 v[160:163], %[voa], %[rs], s62 offen sc1\n" \
    "buffer_load_dwordx4 v[164:167], %[voa], %[rs], s62 offen offset:1024 sc1\n" \
    "buffer_load_dwordx4 v[168:171], %[vob], %[rs], s62 offen sc1\n" \
    "buffer_load_dwordx4 v[172:175], %[vob], %[rs], s62 offen offset:1024 sc1\n" \
    "s_add_u32 s63, %[hoff], 12288\n" \
    "buffer_load_dwordx4 v[176:179], %[voa], %[rs], s63 offen sc1\n" \
    "buffer_load_dwordx4 v[180:183], %[voa], %[rs], s63 offen offset:1024 sc1\n" \
    "buffer_load_dwordx4 v[184:187], %[vob], %[rs], s63 offen sc1\n" \
    "buffer_load_dwordx4 v[188:191], %[vob], %[rs], s63 offen offset:1024 sc1\n" \
    "s_add_u32 s64, %[hoff], 16384\n" \
    "buffer_load_dwordx4 v[192:195], %[voc], %[rs], s64 offen sc1\n" \
    "buffer_load_dwordx4 v[196:199], %[voc], %[rs], s64 offen offset:2048 sc1\n" \
    "buffer_load_dwordx4 v[200:203], %[vod], %[rs], s64 offen sc1\n" \
    "buffer_load_dwordx4 v[204:207], %[vod], %[rs], s64 offen offset:2048 sc1\n" \
    "s_add_u32 s65, %[hoff], 20480\n" \
    "buffer_load_dwordx4 v[208:211], %[voc], %[rs], s65 offen sc1\n" \
    "buffer_load_dwordx4 v[212:215], %[voc], %[rs], s65 offen offset:2048 sc1\n" \
    "buffer_load_dwordx4 v[216:219], %[vod], %[rs], s65 offen sc1\n" \
    "buffer_load_dwordx4 v[220:223], %[vod], %[rs], s65 offen offset:2048 sc1\n" \
    "s_waitcnt vmcnt(20)\n" \
    "v_max3_u32 v224, v128, v129, v130\n" \
    "v_max3_u32 v224, v224, v131, v132\n" \
    "v_max3_u32 v224, v224, v133, v134\n" \
    "v_max3_u32 v224, v224, v135, v136\n" \
    "v_max3_u32 v224, v224, v137, v138\n" \
    "v_max3_u32 v224, v224, v139, v140\n" \
    "v_max3_u32 v224, v224, v141, v142\n" \
    "v_max_u32 v224, v224, v143\n" \
    "v_cmp_eq_u32 vcc, -1, v224\n" \
    "s_cbranch_vccz 300f\n" \
    "s_mov_b32 s68, 0\n" \
    "s_branch 80f\n" \
    "300:\n" \
    "s_add_u32 s60, s60, 24576\n" \
    "v_mfma_f32_16x16x32_bf16 %[c00], v[128:131], a[0:3], 0\n" \
    "v_mfma_f32_16x16x32_bf16 %[c10], v[128:131], a[4:7], 0\n" \
    "v_mfma_f32_16x16x32_bf16 %[c20], v[128:131], a[8:11], 0\n" \
    "v_mfma_f32_16x16x32_bf16 %[c30], v[128:131], a[12:15], 0\n" \
    "v_mfma_f32_16x16x32_bf16 %[c40], v[128:131], a[16:19], 0\n" \
    "buffer_load_dwordx4 v[128:131], %[voa], %[rs], s60 offen sc1\n" \
    "v_mfma_f32_16x16x32_bf16 %[c01], v[132:135], a[0:3], 0\n" \
    "v_mfma_f32_16x16x32_bf16 %[c11], v[132:135], a[4:7], 0\n" \
    "v_mfma_f32_16x16x32_bf16 %[c21], v[132:135], a[8:11], 0\n" \
    "v_mfma_f32_16x16x32_bf16 %[c31], v[132:135], a[12:15], 0\n" \
    "v_mfma_f32_16x16x32_bf16 %[c41], v[132:135], a[16:19], 0\n" \
    "buffer_load_dwordx4 v[132:135], %[voa], %[rs], s60 offen offset:1024 sc1\n" \
    "s_waitcnt vmcnt(18)\n" \
    "v_mfma_f32_16x16x32_bf16 %[c02], v[136:139], a[0:3], 0\n" \
    "v_max3_u32 v224, v144, v145, v146\n" \
    "v_mfma_f32_16x16x32_bf16 %[c12], v[136:139], a[4:7], 0\n" \
    "v_max3_u32 v224, v224, v147, v148\n" \
    "v_mfma_f32_16x16x32_bf16 %[c22], v[136:139], a[8:11], 0\n" \
    "v_max3_u32 v224, v224, v149, v150\n" \
    "v_mfma_f32_16x16x32_bf16 %[c32], v[136:139], a[12:15], 0\n" \
    "v_max3_u32 v224, v224, v151, v152\n" \
    "v_mfma_f32_16x16x32_bf16 %[c42], v[136:139], a[16:19], 0\n" \
    "v_max3_u32 v224, v224, v153, v154\n" \
    "buffer_load_dwordx4 v[136:139], %[vob], %[rs], s60 offen sc1\n" \
    "v_mfma_f32_16x16x32_bf16 %[c03], v[140:143], a[0:3], 0\n" \
    "v_max3_u32 v224, v224, v155, v156\n" \
    "v_mfma_f32_16x16x32_bf16 %[c13], v[140:143], a[4:7], 0\n" \
    "v_max3_u32 v224, v224, v157, v158\n" \
    "v_mfma_f32_16x16x32_bf16 %[c23], v[140:143], a[8:11], 0\n" \
    "v_max_u32 v224, v224, v159\n" \
    "v_mfma_f32_16x16x32_bf16 %[c33], v[140:143], a[12:15], 0\n" \
    "v_cmp_eq_u32 vcc, -1, v224\n" \
    "v_mfma_f32_16x16x32_bf16 %[c43], v[140:143], a[16:19], 0\n" \
    "buffer_load_dwordx4 v[140:143], %[vob], %[rs], s60 offen offset:1024 sc1\n" \
    "s_cbranch_vccz 301f\n" \
    "s_mov_b32 s68, 1\n" \
    "s_branch 80f\n" \
    "301:\n" \
    "s_add_u32 s61, s61, 24576\n" \
    "v_mfma_f32_16x16x32_bf16 %[c00], v[144:147], a[20:23], %[c00]\n" \
    "v_mfma_f32_16x16x32_bf16 %[c10], v[144:147], a[24:27], %[c10]\n" \
    "v_mfma_f32_16x16x32_bf16 %[c20], v[144:147], a[28:31], %[c20]\n" \
    "v_mfma_f32_16x16x32_bf16 %[c30], v[144:147], a[32:35], %[c30]\n" \
    "v_mfma_f32_16x16x32_bf16 %[c40], v[144:147], a[36:39], %[c40]\n" \
    "buffer_load_dwordx4 v[144:147], %[voa], %[rs], s61 offen sc1\n" \
    "v_mfma_f32_16x16x32_bf16 %[c01], v[148:151], a[20:23], %[c01]\n" \
    "v_mfma_f32_16x16x32_bf16 %[c11], v[148:151], a[24:27], %[c11]\n" \
    "v_mfma_f32_16x16x32_bf16 %[c21], v[148:151], a[28:31], %[c21]\n" \
    "v_mfma_f32_16x16x32_bf16 %[c31], v[148:151], a[32:35], %[c31]\n" \
    "v_mfma_f32_16x16x32_bf16 %[c41], v[148:151], a[36:39], %[c41]\n" \
    "buffer_load_dwordx4 v[148:151], %[voa], %[rs], s61 offen offset:1024 sc1\n" \
    "s_waitcnt vmcnt(18)\n" \
    "v_mfma_f32_16x16x32_bf16 %[c02], v[152:155], a[20:23], %[c02]\n" \
    "v_max3_u32 v224, v160, v161, v162\n" \
    "v_mfma_f32_16x16x32_bf16 %[c12], v[152:155], a[24:27], %[c12]\n" \
    "v_max3_u32 v224, v224, v163, v164\n" \
    "v_mfma_f32_16x16x32_bf16 %[c22], v[152:155], a[28:31], %[c22]\n" \
    "v_max3_u32 v224, v224, v165, v166\n" \
    "v_mfma_f32_16x16x32_bf16 %[c32], v[152:155], a[32:35], %[c32]\n" \
    "v_max3_u32 v224, v224, v167, v168\n" \
    "v_mfma_f32_16x16x32_bf16 %[c42], v[152:155], a[36:39], %[c42]\n" \
    "v_max3_u32 v224, v224, v169, v170\n" \
    "buffer_load_dwordx4 v[152:155], %[vob], %[rs], s61 offen sc1\n" \
    "v_mfma_f32_16x16x32_bf16 %[c03], v[156:159], a[20:23], %[c03]\n" \
    "v_max3_u32 v224, v224, v171, v172\n" \
    "v_mfma_f32_16x16x32_bf16 %[c13], v[156:159], a[24:27], %[c13]\n" \
    "v_max3_u32 v224, v224, v173, v174\n" \
    "v_mfma_f32_16x16x32_bf16 %[c23], v[156:159], a[28:31], %[c23]\n" \
    "v_max_u32 v224, v224, v175\n" \
    "v_mfma_f32_16x16x32_bf16 %[c33], v[156:159], a[32:35], %[c33]\n" \
    "v_cmp_eq_u32 vcc, -1, v224\n" \
    "v_mfma_f32_16x16x32_bf16 %[c43], v[156:159], a[36:39], %[c43]\n" \
    "buffer_load_dwordx4 v[156:159], %[vob], %[rs], s61 offen offset:1024 sc1\n" \
    "s_cbranch_vccz 302f\n" \
    "s_mov_b32 s68, 2\n" \
    "s_branch 80f\n" \
    "302:\n" \
    "s_add_u32 s62, s62, 24576\n" \
    "v_mfma_f32_16x16x32_bf16 %[c00], v[160:163], a[40:43], %[c00]\n" \
    "v_mfma_f32_16x16x32_bf16 %[c10], v[160:163], a[44:47], %[c10]\n" \
    "v_mfma_f32_16x16x32_bf16 %[c20], v[160:163], a[48:51], %[c20]\n" \
    "v_mfma_f32_16x16x32_bf16 %[c30], v[160:163], a[52:55], %[c30]\n" \
    "v_mfma_f32_16x16x32_bf16 %[c40], v[160:163], a[56:59], %[c40]\n" \
    "buffer_load_dwordx4 v[160:163], %[voa], %[rs], s62 offen sc1\n" \
    "v_mfma_f32_16x16x32_bf16 %[c01], v[164:167], a[40:43], %[c01]\n" \
    "v_mfma_f32_16x16x32_bf16 %[c11], v[164:167], a[44:47], %[c11]\n" \
    "v_mfma_f32_16x16x32_bf16 %[c21], v[164:167], a[48:51], %[c21]\n" \
    "v_mfma_f32_16x16x32_bf16 %[c31], v[164:167], a[52:55], %[c31]\n" \
    "v_mfma_f32_16x16x32_bf16 %[c41], v[164:167], a[56:59], %[c41]\n" \
    "buffer_load_dwordx4 v[164:167], %[voa], %[rs], s62 offen offset:1024 sc1\n" \
    "s_waitcnt vmcnt(18)\n" \
    "v_mfma_f32_16x16x32_bf16 %[c02], v[168:171], a[40:43], %[c02]\n" \
    "v_max3_u32 v224, v176, v177, v178\n" \
    "v_mfma_f32_16x16x32_bf16 %[c12], v[168:171], a[44:47], %[c12]\n" \
    "v_max3_u32 v224, v224, v179, v180\n" \
    "v_mfma_f32_16x16x32_bf16 %[c22], v[168:171], a[48:51], %[c22]\n" \
    "v_max3_u32 v224, v224, v181, v182\n" \
    "v_mfma_f32_16x16x32_bf16 %[c32], v[168:171], a[52:55], %[c32]\n" \
    "v_max3_u32 v224, v224, v183, v184\n" \
    "v_mfma_f32_16x16x32_bf16 %[c42], v[168:171], a[56:59], %[c42]\n" \
    "v_max3_u32 v224, v224, v185, v186\n" \
    "buffer_load_dwordx4 v[168:171], %[vob], %[rs], s62 offen sc1\n" \
    "v_mfma_f32_16x16x32_bf16 %[c03], v[172:175], a[40:43], %[c03]\n" \
    "v_max3_u32 v224, v224, v187, v188\n" \
    "v_mfma_f32_16x16x32_bf16 %[c13], v[172:175], a[44:47], %[c13]\n" \
    "v_max3_u32 v224, v224, v189, v190\n" \
    "v_mfma_f32_16x16x32_bf16 %[c23], v[172:175], a[48:51], %[c23]\n" \
    "v_max_u32 v224, v224, v191\n" \
    "v_mfma_f32_16x16x32_bf16 %[c33], v[172:175], a[52:55], %[c33]\n" \
    "v_cmp_eq_u32 vcc, -1, v224\n" \
    "v_mfma_f32_16x16x32_bf16 %[c43], v[172:175], a[56:59], %[c43]\n" \
    "buffer_load_dwordx4 v[172:175], %[vob], %[rs], s62 offen offset:1024 sc1\n" \
    "s_cbranch_vccz 303f\n" \
    "s_mov_b32 s68, 3\n" \
    "s_branch 80f\n" \
    "303:\n" \
    "s_add_u32 s63, s63, 24576\n" \
    "v_mfma_f32_16x16x32_bf16 %[c00], v[176:179], a[60:63], %[c00]\n" \
    "v_mfma_f32_16x16x32_bf16 %[c10], v[176:179], a[64:67], %[c10]\n" \
    "v_mfma_f32_16x16x32_bf16 %[c20], v[176:179], a[68:71], %[c20]\n" \
    "v_mfma_f32_16x16x32_bf16 %[c30], v[176:179], a[72:75], %[c30]\n" \
    "v_mfma_f32_16x16x32_bf16 %[c40], v[176:179], a[76:79], %[c40]\n" \
    "buffer_load_dwordx4 v[176:179], %[voa], %[rs], s63 offen sc1\n" \
    "v_mfma_f32_16x16x32_bf16 %[c01], v[180:183], a[60:63], %[c01]\n" \
    "v_mfma_f32_16x16x32_bf16 %[c11], v[180:183], a[64:67], %[c11]\n" \
    "v_mfma_f32_16x16x32_bf16 %[c21], v[180:183], a[68:71], %[c21]\n" \
    "v_mfma_f32_16x16x32_bf16 %[c31], v[180:183], a[72:75], %[c31]\n" \
    "v_mfma_f32_16x16x32_bf16 %[c41], v[180:183], a[76:79], %[c41]\n" \
    "buffer_load_dwordx4 v[180:183], %[voa], %[rs], s63 offen offset:1024 sc1\n" \
    "s_waitcnt vmcnt(18)\n" \
    "v_mfma_f32_16x16x32_bf16 %[c02], v[184:187], a[60:63], %[c02]\n" \
    "v_max3_u32 v224, v192, v193, v194\n" \
    "v_mfma_f32_16x16x32_bf16 %[c12], v[184:187], a[64:67], %[c12]\n" \
    "v_max3_u32 v224, v224, v195, v196\n" \
    "v_mfma_f32_16x16x32_bf16 %[c22], v[184:187], a[68:71], %[c22]\n" \
    "v_max3_u32 v224, v224, v197, v198\n" \
    "v_mfma_f32_16x16x32_bf16 %[c32], v[184:187], a[72:75], %[c32]\n" \
    "v_max3_u32 v224, v224, v199, v200\n" \
    "v_mfma_f32_16x16x32_bf16 %[c42], v[184:187], a[76:79], %[c42]\n" \
    "v_max3_u32 v224, v224, v201, v202\n" \
    "buffer_load_dwordx4 v[184:187], %[vob], %[rs], s63 offen sc1\n" \
    "v_mfma_f32_16x16x32_bf16 %[c03], v[188:191], a[60:63], %[c03]\n" \
    "v_max3_u32 v224, v224, v203, v204\n" \
    "v_mfma_f32_16x16x32_bf16 %[c13], v[188:191], a[64:67], %[c13]\n" \
    "v_max3_u32 v224, v224, v205, v206\n" \
    "v_mfma_f32_16x16x32_bf16 %[c23], v[188:191], a[68:71], %[c23]\n" \
    "v_max_u32 v224, v224, v207\n" \
    "v_mfma_f32_16x16x32_bf16 %[c33], v[188:191], a[72:75], %[c33]\n" \
    "v_cmp_eq_u32 vcc, -1, v224\n" \
    "v_mfma_f32_16x16x32_bf16 %[c43], v[188:191], a[76:79], %[c43]\n" \
    "buffer_load_dwordx4 v[188:191], %[vob], %[rs], s63 offen offset:1024 sc1\n" \
    "s_cbranch_vccz 304f\n" \
    "s_mov_b32 s68, 4\n" \
    "s_branch 80f\n" \
    "304:\n" \
    "v_swap_b32 v194, v196\n" \
    "v_swap_b32 v195, v197\n" \
    "v_swap_b32 v202, v204\n" \
    "v_swap_b32 v203, v205\n" \
    "s_nop 1\n" \
    "s_add_u32 s64, s64, 24576\n" \
    "v_mfma_f32_16x16x32_bf16 %[c00], v[192:195], a[80:83], %[c00]\n" \
    "v_mfma_f32_16x16x32_bf16 %[c10], v[192:195], a[84:87], %[c10]\n" \
    "v_mfma_f32_16x16x32_bf16 %[c20], v[192:195], a[88:91], %[c20]\n" \
    "v_mfma_f32_16x16x32_bf16 %[c30], v[192:195], a[92:95], %[c30]\n" \
    "v_mfma_f32_16x16x32_bf16 %[c40], v[192:195], a[96:99], %[c40]\n" \
    "buffer_load_dwordx4 v[192:195], %[voc], %[rs], s64 offen sc1\n" \
    "v_mfma_f32_16x16x32_bf16 %[c01], v[196:199], a[80:83], %[c01]\n" \
    "v_mfma_f32_16x16x32_bf16 %[c11], v[196:199], a[84:87], %[c11]\n" \
    "v_mfma_f32_16x16x32_bf16 %[c21], v[196:199], a[88:91], %[c21]\n" \
    "v_mfma_f32_16x16x32_bf16 %[c31], v[196:199], a[92:95], %[c31]\n" \
    "v_mfma_f32_16x16x32_bf16 %[c41], v[196:199], a[96:99], %[c41]\n" \
    "buffer_load_dwordx4 v[196:199], %[voc], %[rs], s64 offen offset:2048 sc1\n" \
    "s_waitcnt vmcnt(18)\n" \
    "v_mfma_f32_16x16x32_bf16 %[c02], v[200:203], a[80:83], %[c02]\n" \
    "v_max3_u32 v224, v208, v209, v210\n" \
    "v_mfma_f32_16x16x32_bf16 %[c12], v[200:203], a[84:87], %[c12]\n" \
    "v_max3_u32 v224, v224, v211, v212\n" \
    "v_mfma_f32_16x16x32_bf16 %[c22], v[200:203], a[88:91], %[c22]\n" \
    "v_max3_u32 v224, v224, v213, v214\n" \
    "v_mfma_f32_16x16x32_bf16 %[c32], v[200:203], a[92:95], %[c32]\n" \
    "v_max3_u32 v224, v224, v215, v216\n" \
    "v_mfma_f32_16x16x32_bf16 %[c42], v[200:203], a[96:99], %[c42]\n" \
    "v_max3_u32 v224, v224, v217, v218\n" \
    "buffer_load_dwordx4 v[200:203], %[vod], %[rs], s64 offen sc1\n" \
    "v_mfma_f32_16x16x32_bf16 %[c03], v[204:207], a[80:83], %[c03]\n" \
    "v_max3_u32 v224, v224, v219, v220\n" \
    "v_mfma_f32_16x16x32_bf16 %[c13], v[204:207], a[84:87], %[c13]\n" \
    "v_max3_u32 v224, v224, v221, v222\n" \
    "v_mfma_f32_16x16x32_bf16 %[c23], v[204:207], a[88:91], %[c23]\n" \
    "v_max_u32 v224, v224, v223\n" \
    "v_mfma_f32_16x16x32_bf16 %[c33], v[204:207], a[92:95], %[c33]\n" \
    "v_cmp_eq_u32 vcc, -1, v224\n" \
    "v_mfma_f32_16x16x32_bf16 %[c43], v[204:207], a[96:99], %[c43]\n" \
    "buffer_load_dwordx4 v[204:207], %[vod], %[rs], s64 offen offset:2048 sc1\n" \
    "s_cbranch_vccz 305f\n" \
    "s_mov_b32 s68, 5\n" \
    "s_branch 80f\n" \
    "305:\n" \
    "v_swap_b32 v210, v212\n" \
    "v_swap_b32 v211, v213\n" \
    "v_swap_b32 v218, v220\n" \
    "v_swap_b32 v219, v221\n" \
    "s_nop 1\n" \
    "s_add_u32 s65, s65, 24576\n" \
    "v_mfma_f32_16x16x32_bf16 %[c00], v[208:211], a[100:103], %[c00]\n" \
    "v_mfma_f32_16x16x32_bf16 %[c10], v[208:211], a[104:107], %[c10]\n" \
    "v_mfma_f32_16x16x32_bf16 %[c20], v[208:211], a[108:111], %[c20]\n" \
    "v_mfma_f32_16x16x32_bf16 %[c30], v[208:211], a[112:115], %[c30]\n" \
    "v_mfma_f32_16x16x32_bf16 %[c40], v[208:211], a[116:119], %[c40]\n" \
    "buffer_load_dwordx4 v[208:211], %[voc], %[rs], s65 offen sc1\n" \
    "v_mfma_f32_16x16x32_bf16 %[c01], v[212:215], a[100:103], %[c01]\n" \
    "v_mfma_f32_16x16x32_bf16 %[c11], v[212:215], a[104:107], %[c11]\n" \
    "v_mfma_f32_16x16x32_bf16 %[c21], v[212:215], a[108:111], %[c21]\n" \
    "v_mfma_f32_16x16x32_bf16 %[c31], v[212:215], a[112:115], %[c31]\n" \
    "v_mfma_f32_16x16x32_bf16 %[c41], v[212:215], a[116:119], %[c41]\n" \
    "buffer_load_dwordx4 v[212:215], %[voc], %[rs], s65 offen offset:2048 sc1\n" \
    "s_waitcnt vmcnt(18)\n" \
    "v_mfma_f32_16x16x32_bf16 %[c02], v[216:219], a[100:103], %[c02]\n" \
    "v_max3_u32 v224, v128, v129, v130\n" \
    "v_mfma_f32_16x16x32_bf16 %[c12], v[216:219], a[104:107], %[c12]\n" \
    "v_max3_u32 v224, v224, v131, v132\n" \
    "v_mfma_f32_16x16x32_bf16 %[c22], v[216:219], a[108:111], %[c22]\n" \
    "v_max3_u32 v224, v224, v133, v134\n" \
    "v_mfma_f32_16x16x32_bf16 %[c32], v[216:219], a[112:115], %[c32]\n" \
    "v_max3_u32 v224, v224, v135, v136\n" \
    "v_mfma_f32_16x16x32_bf16 %[c42], v[216:219], a[116:119], %[c42]\n" \
    "v_max3_u32 v224, v224, v137, v138\n" \
    "buffer_load_dwordx4 v[216:219], %[vod], %[rs], s65 offen sc1\n" \
    "v_mfma_f32_16x16x32_bf16 %[c03], v[220:223], a[100:103], %[c03]\n" \
    "v_max3_u32 v224, v224, v139, v140\n" \
    "v_mfma_f32_16x16x32_bf16 %[c13], v[220:223], a[104:107], %[c13]\n" \
    "v_max3_u32 v224, v224, v141, v142\n" \
    "v_mfma_f32_16x16x32_bf16 %[c23], v[220:223], a[108:111], %[c23]\n" \
    "v_max_u32 v224, v224, v143\n" \
    "v_mfma_f32_16x16x32_bf16 %[c33], v[220:223], a[112:115], %[c33]\n" \
    "v_cmp_eq_u32 vcc, -1, v224\n" \
    "v_mfma_f32_16x16x32_bf16 %[c43], v[220:223], a[116:119], %[c43]\n" \
    "buffer_load_dwordx4 v[220:223], %[vod], %[rs], s65 offen offset:2048 sc1\n" \
    "s_cbranch_vccz 306f\n" \
    "s_mov_b32 s68, 6\n" \
    "s_branch 80f\n" \
    "306:\n" \
    "v_mfma_f32_16x16x32_bf16 %[c00], v[128:131], a[120:123], %[c00]\n" \
    "v_mfma_f32_16x16x32_bf16 %[c10], v[128:131], a[124:127], %[c10]\n" \
    "v_mfma_f32_16x16x32_bf16 %[c20], v[128:131], a[128:131], %[c20]\n" \
    "v_mfma_f32_16x16x32_bf16 %[c30], v[128:131], a[132:135], %[c30]\n" \
    "v_mfma_f32_16x16x32_bf16 %[c40], v[128:131], a[136:139], %[c40]\n" \
    "s_add_u32 s69, %[h1off], 0\n" \
    "buffer_store_dwordx4 v[228:231], %[vh1], %[rs], s69 offen sc1\n" \
    "v_mfma_f32_16x16x32_bf16 %[c01], v[132:135], a[120:123], %[c01]\n" \
    "v_mfma_f32_16x16x32_bf16 %[c11], v[132:135], a[124:127], %[c11]\n" \
    "v_mfma_f32_16x16x32_bf16 %[c21], v[132:135], a[128:131], %[c21]\n" \
    "v_mfma_f32_16x16x32_bf16 %[c31], v[132:135], a[132:135], %[c31]\n" \
    "v_mfma_f32_16x16x32_bf16 %[c41], v[132:135], a[136:139], %[c41]\n" \
    "s_waitcnt vmcnt(17)\n" \
    "v_mfma_f32_16x16x32_bf16 %[c02], v[136:139], a[120:123], %[c02]\n" \
    "v_max3_u32 v224, v144, v145, v146\n" \
    "v_mfma_f32_16x16x32_bf16 %[c12], v[136:139], a[124:127], %[c12]\n" \
    "v_max3_u32 v224, v224, v147, v148\n" \
    "v_mfma_f32_16x16x32_bf16 %[c22], v[136:139], a[128:131], %[c22]\n" \
    "v_max3_u32 v224, v224, v149, v150\n" \
    "v_mfma_f32_16x16x32_bf16 %[c32], v[136:139], a[132:135], %[c32]\n" \
    "v_max3_u32 v224, v224, v151, v152\n" \
    "v_mfma_f32_16x16x32_bf16 %[c42], v[136:139], a[136:139], %[c42]\n" \
    "v_max3_u32 v224, v224, v153, v154\n" \
    "s_add_u32 s69, %[h1off], 4096\n" \
    "buffer_store_dwordx4 v[228:231], %[vh1], %[rs], s69 offen sc1\n" \
    "v_mfma_f32_16x16x32_bf16 %[c03], v[140:143], a[120:123], %[c03]\n" \
    "v_max3_u32 v224, v224, v155, v156\n" \
    "v_mfma_f32_16x16x32_bf16 %[c13], v[140:143], a[124:127], %[c13]\n" \
    "v_max3_u32 v224, v224, v157, v158\n" \
    "v_mfma_f32_16x16x32_bf16 %[c23], v[140:143], a[128:131], %[c23]\n" \
    "v_max_u32 v224, v224, v159\n" \
    "v_mfma_f32_16x16x32_bf16 %[c33], v[140:143], a[132:135], %[c33]\n" \
    "v_cmp_eq_u32 vcc, -1, v224\n" \
    "v_mfma_f32_16x16x32_bf16 %[c43], v[140:143], a[136:139], %[c43]\n" \
    "s_cbranch_vccz 307f\n" \
    "s_mov_b32 s68, 7\n" \
    "s_branch 80f\n" \
    "307:\n" \
    "v_mfma_f32_16x16x32_bf16 %[c00], v[144:147], a[140:143], %[c00]\n" \
    "v_mfma_f32_16x16x32_bf16 %[c10], v[144:147], a[144:147], %[c10]\n" \
    "v_mfma_f32_16x16x32_bf16 %[c20], v[144:147], a[148:151], %[c20]\n" \
    "v_mfma_f32_16x16x32_bf16 %[c30], v[144:147], a[152:155], %[c30]\n" \
    "v_mfma_f32_16x16x32_bf16 %[c40], v[144:147], a[156:159], %[c40]\n" \
    "s_add_u32 s69, %[h1off], 8192\n" \
    "buffer_store_dwordx4 v[228:231], %[vh1], %[rs], s69 offen sc1\n" \
    "v_mfma_f32_16x16x32_bf16 %[c01], v[148:151], a[140:143], %[c01]\n" \
    "v_mfma_f32_16x16x32_bf16 %[c11], v[148:151], a[144:147], %[c11]\n" \
    "v_mfma_f32_16x16x32_bf16 %[c21], v[148:151], a[148:151], %[c21]\n" \
    "v_mfma_f32_16x16x32_bf16 %[c31], v[148:151], a[152:155], %[c31]\n" \
    "v_mfma_f32_16x16x32_bf16 %[c41], v[148:151], a[156:159], %[c41]\n" \
    "s_waitcnt vmcnt(15)\n" \
    "v_mfma_f32_16x16x32_bf16 %[c02], v[152:155], a[140:143], %[c02]\n" \
    "v_max3_u32 v224, v160, v161, v162\n" \
    "v_mfma_f32_16x16x32_bf16 %[c12], v[152:155], a[144:147], %[c12]\n" \
    "v_max3_u32 v224, v224, v163, v164\n" \
    "v_mfma_f32_16x16x32_bf16 %[c22], v[152:155], a[148:151], %[c22]\n" \
    "v_max3_u32 v224, v224, v165, v166\n" \
    "v_mfma_f32_16x16x32_bf16 %[c32], v[152:155], a[152:155], %[c32]\n" \
    "v_max3_u32 v224, v224, v167, v168\n" \
    "v_mfma_f32_16x16x32_bf16 %[c42], v[152:155], a[156:159], %[c42]\n" \
    "v_max3_u32 v224, v224, v169, v170\n" \
    "s_add_u32 s69, %[h1off], 12288\n" \
    "buffer_store_dwordx4 v[228:231], %[vh1], %[rs], s69 offen sc1\n" \
    "v_mfma_f32_16x16x32_bf16 %[c03], v[156:159], a[140:143], %[c03]\n" \
    "v_max3_u32 v224, v224, v171, v172\n" \
    "v_mfma_f32_16x16x32_bf16 %[c13], v[156:159], a[144:147], %[c13]\n" \
    "v_max3_u32 v224, v224, v173, v174\n" \
    "v_mfma_f32_16x16x32_bf16 %[c23], v[156:159], a[148:151], %[c23]\n" \
    "v_max_u32 v224, v224, v175\n" \
    "v_mfma_f32_16x16x32_bf16 %[c33], v[156:159], a[152:155], %[c33]\n" \
    "v_cmp_eq_u32 vcc, -1, v224\n" \
    "v_mfma_f32_16x16x32_bf16 %[c43], v[156:159], a[156:159], %[c43]\n" \
    "s_cbranch_vccz 308f\n" \
    "s_mov_b32 s68, 8\n" \
    "s_branch 80f\n" \
    "308:\n" \
    "v_mfma_f32_16x16x32_bf16 %[c00], v[160:163], a[160:163], %[c00]\n" \
    "v_mfma_f32_16x16x32_bf16 %[c10], v[160:163], a[164:167], %[c10]\n" \
    "v_mfma_f32_16x16x32_bf16 %[c20], v[160:163], a[168:171], %[c20]\n" \
    "v_mfma_f32_16x16x32_bf16 %[c30], v[160:163], a[172:175], %[c30]\n" \
    "v_mfma_f32_16x16x32_bf16 %[c40], v[160:163], a[176:179], %[c40]\n" \
    "s_add_u32 s69, %[h1off], 16384\n" \
    "buffer_store_dwordx4 v[228:231], %[vh1], %[rs], s69 offen sc1\n" \
    "v_mfma_f32_16x16x32_bf16 %[c01], v[164:167], a[160:163], %[c01]\n" \
    "v_mfma_f32_16x16x32_bf16 %[c11], v[164:167], a[164:167], %[c11]\n" \
    "v_mfma_f32_16x16x32_bf16 %[c21], v[164:167], a[168:171], %[c21]\n" \
    "v_mfma_f32_16x16x32_bf16 %[c31], v[164:167], a[172:175], %[c31]\n" \
    "v_mfma_f32_16x16x32_bf16 %[c41], v[164:167], a[176:179], %[c41]\n" \
    "s_waitcnt vmcnt(13)\n" \
    "v_mfma_f32_16x16x32_bf16 %[c02], v[168:171], a[160:163], %[c02]\n" \
    "v_max3_u32 v224, v176, v177, v178\n" \
    "v_mfma_f32_16x16x32_bf16 %[c12], v[168:171], a[164:167], %[c12]\n" \
    "v_max3_u32 v224, v224, v179, v180\n" \
    "v_mfma_f32_16x16x32_bf16 %[c22], v[168:171], a[168:171], %[c22]\n" \
    "v_max3_u32 v224, v224, v181, v182\n" \
    "v_mfma_f32_16x16x32_bf16 %[c32], v[168:171], a[172:175], %[c32]\n" \
    "v_max3_u32 v224, v224, v183, v184\n" \
    "v_mfma_f32_16x16x32_bf16 %[c42], v[168:171], a[176:179], %[c42]\n" \
    "v_max3_u32 v224, v224, v185, v186\n" \
    "s_add_u32 s69, %[h1off], 20480\n" \
    "buffer_store_dwordx4 v[228:231], %[vh1], %[rs], s69 offen sc1\n" \
    "v_mfma_f32_16x16x32_bf16 %[c03], v[172:175], a[160:163], %[c03]\n" \
    "v_max3_u32 v224, v224, v187, v188\n" \
    "v_mfma_f32_16x16x32_bf16 %[c13], v[172:175], a[164:167], %[c13]\n" \
    "v_max3_u32 v224, v224, v189, v190\n" \
    "v_mfma_f32_16x16x32_bf16 %[c23], v[172:175], a[168:171], %[c23]\n" \
    "v_max_u32 v224, v224, v191\n" \
    "v_mfma_f32_16x16x32_bf16 %[c33], v[172:175], a[172:175], %[c33]\n" \
    "v_cmp_eq_u32 vcc, -1, v224\n" \
    "v_mfma_f32_16x16x32_bf16 %[c43], v[172:175], a[176:179], %[c43]\n" \
    "s_cbranch_vccz 309f\n" \
    "s_mov_b32 s68, 9\n" \
    "s_branch 80f\n" \
    "309:\n" \
    "v_mfma_f32_16x16x32_bf16 %[c00], v[176:179], a[180:183], %[c00]\n" \
    "v_mfma_f32_16x16x32_bf16 %[c10], v[176:179], a[184:187], %[c10]\n" \
    "v_mfma_f32_16x16x32_bf16 %[c20], v[176:179], a[188:191], %[c20]\n" \
    "v_mfma_f32_16x16x32_bf16 %[c30], v[176:179], a[192:195], %[c30]\n" \
    "v_mfma_f32_16x16x32_bf16 %[c40], v[176:179], a[196:199], %[c40]\n" \
    "s_add_u32 s69, %[x3off], 0\n" \
    "buffer_store_dwordx4 v[228:231], %[vx3], %[rs], s69 offen sc1\n" \
    "v_mfma_f32_16x16x32_bf16 %[c01], v[180:183], a[180:183], %[c01]\n" \
    "v_mfma_f32_16x16x32_bf16 %[c11], v[180:183], a[184:187], %[c11]\n" \
    "v_mfma_f32_16x16x32_bf16 %[c21], v[180:183], a[188:191], %[c21]\n" \
    "v_mfma_f32_16x16x32_bf16 %[c31], v[180:183], a[192:195], %[c31]\n" \
    "v_mfma_f32_16x16x32_bf16 %[c41], v[180:183], a[196:199], %[c41]\n" \
    "s_waitcnt vmcnt(11)\n" \
    "v_mfma_f32_16x16x32_bf16 %[c02], v[184:187], a[180:183], %[c02]\n" \
    "v_max3_u32 v224, v192, v193, v194\n" \
    "v_mfma_f32_16x16x32_bf16 %[c12], v[184:187], a[184:187], %[c12]\n" \
    "v_max3_u32 v224, v224, v195, v196\n" \
    "v_mfma_f32_16x16x32_bf16 %[c22], v[184:187], a[188:191], %[c22]\n" \
    "v_max3_u32 v224, v224, v197, v198\n" \
    "v_mfma_f32_16x16x32_bf16 %[c32], v[184:187], a[192:195], %[c32]\n" \
    "v_max3_u32 v224, v224, v199, v200\n" \
    "v_mfma_f32_16x16x32_bf16 %[c42], v[184:187], a[196:199], %[c42]\n" \
    "v_max3_u32 v224, v224, v201, v202\n" \
    "s_add_u32 s69, %[x3off], 131072\n" \
    "buffer_store_dwordx4 v[228:231], %[vx3], %[rs], s69 offen sc1\n" \
    "v_mfma_f32_16x16x32_bf16 %[c03], v[188:191], a[180:183], %[c03]\n" \
    "v_max3_u32 v224, v224, v203, v204\n" \
    "v_mfma_f32_16x16x32_bf16 %[c13], v[188:191], a[184:187], %[c13]\n" \
    "v_max3_u32 v224, v224, v205, v206\n" \
    "v_mfma_f32_16x16x32_bf16 %[c23], v[188:191], a[188:191], %[c23]\n" \
    "v_max_u32 v224, v224, v207\n" \
    "v_mfma_f32_16x16x32_bf16 %[c33], v[188:191], a[192:195], %[c33]\n" \
    "v_cmp_eq_u32 vcc, -1, v224\n" \
    "v_mfma_f32_16x16x32_bf16 %[c43], v[188:191], a[196:199], %[c43]\n" \
    "s_cbranch_vccz 310f\n" \
    "s_mov_b32 s68, 10\n" \
    "s_branch 80f\n" \
    "310:\n" \
    "v_swap_b32 v194, v196\n" \
    "v_swap_b32 v195, v197\n" \
    "v_swap_b32 v202, v204\n" \
    "v_swap_b32 v203, v205\n" \
    "s_nop 1\n" \
    "v_mfma_f32_16x16x32_bf16 %[c00], v[192:195], a[200:203], %[c00]\n" \
    "v_mfma_f32_16x16x32_bf16 %[c10], v[192:195], a[204:207], %[c10]\n" \
    "v_mfma_f32_16x16x32_bf16 %[c20], v[192:195], a[208:211], %[c20]\n" \
    "v_mfma_f32_16x16x32_bf16 %[c30], v[192:195], a[212:215], %[c30]\n" \
    "v_mfma_f32_16x16x32_bf16 %[c40], v[192:195], a[216:219], %[c40]\n" \
    "s_add_u32 s69, %[x3off], 0\n" \
    "buffer_store_dwordx4 v[228:231], %[vx4], %[rs], s69 offen sc1\n" \
    "v_mfma_f32_16x16x32_bf16 %[c01], v[196:199], a[200:203], %[c01]\n" \
    "v_mfma_f32_16x16x32_bf16 %[c11], v[196:199], a[204:207], %[c11]\n" \
    "v_mfma_f32_16x16x32_bf16 %[c21], v[196:199], a[208:211], %[c21]\n" \
    "v_mfma_f32_16x16x32_bf16 %[c31], v[196:199], a[212:215], %[c31]\n" \
    "v_mfma_f32_16x16x32_bf16 %[c41], v[196:199], a[216:219], %[c41]\n" \
    "s_waitcnt vmcnt(9)\n" \
    "v_mfma_f32_16x16x32_bf16 %[c02], v[200:203], a[200:203], %[c02]\n" \
    "v_max3_u32 v224, v208, v209, v210\n" \
    "v_mfma_f32_16x16x32_bf16 %[c12], v[200:203], a[204:207], %[c12]\n" \
    "v_max3_u32 v224, v224, v211, v212\n" \
    "v_mfma_f32_16x16x32_bf16 %[c22], v[200:203], a[208:211], %[c22]\n" \
    "v_max3_u32 v224, v224, v213, v214\n" \
    "v_mfma_f32_16x16x32_bf16 %[c32], v[200:203], a[212:215], %[c32]\n" \
    "v_max3_u32 v224, v224, v215, v216\n" \
    "v_mfma_f32_16x16x32_bf16 %[c42], v[200:203], a[216:219], %[c42]\n" \
    "v_max3_u32 v224, v224, v217, v218\n" \
    "v_mfma_f32_16x16x32_bf16 %[c03], v[204:207], a[200:203], %[c03]\n" \
    "v_max3_u32 v224, v224, v219, v220\n" \
    "v_mfma_f32_16x16x32_bf16 %[c13], v[204:207], a[204:207], %[c13]\n" \
    "v_max3_u32 v224, v224, v221, v222\n" \
    "v_mfma_f32_16x16x32_bf16 %[c23], v[204:207], a[208:211], %[c23]\n" \
    "v_max_u32 v224, v224, v223\n" \
    "v_mfma_f32_16x16x32_bf16 %[c33], v[204:207], a[212:215], %[c33]\n" \
    "v_cmp_eq_u32 vcc, -1, v224\n" \
    "v_mfma_f32_16x16x32_bf16 %[c43], v[204:207], a[216:219], %[c43]\n" \
    "s_cbranch_vccz 311f\n" \
    "s_mov_b32 s68, 11\n" \
    "s_branch 80f\n" \
    "311:\n" \
    "v_swap_b32 v210, v212\n" \
    "v_swap_b32 v211, v213\n" \
    "v_swap_b32 v218, v220\n" \
    "v_swap_b32 v219, v221\n" \
    "s_nop 1\n" \
    "v_mfma_f32_16x16x32_bf16 %[c00], v[208:211], a[220:223], %[c00]\n" \
    "v_mfma_f32_16x16x32_bf16 %[c10], v[208:211], a[224:227], %[c10]\n" \
    "v_mfma_f32_16x16x32_bf16 %[c20], v[208:211], a[228:231], %[c20]\n" \
    "v_mfma_f32_16x16x32_bf16 %[c30], v[208:211], a[232:235], %[c30]\n" \
    "v_mfma_f32_16x16x32_bf16 %[c40], v[208:211], a[236:239], %[c40]\n" \
    "v_mfma_f32_16x16x32_bf16 %[c01], v[212:215], a[220:223], %[c01]\n" \
    "v_mfma_f32_16x16x32_bf16 %[c11], v[212:215], a[224:227], %[c11]\n" \
    "v_mfma_f32_16x16x32_bf16 %[c21], v[212:215], a[228:231], %[c21]\n" \
    "v_mfma_f32_16x16x32_bf16 %[c31], v[212:215], a[232:235], %[c31]\n" \
    "v_mfma_f32_16x16x32_bf16 %[c41], v[212:215], a[236:239], %[c41]\n" \
    "v_mfma_f32_16x16x32_bf16 %[c02], v[216:219], a[220:223], %[c02]\n" \
    "v_mfma_f32_16x16x32_bf16 %[c12], v[216:219], a[224:227], %[c12]\n" \
    "v_mfma_f32_16x16x32_bf16 %[c22], v[216:219], a[228:231], %[c22]\n" \
    "v_mfma_f32_16x16x32_bf16 %[c32], v[216:219], a[232:235], %[c32]\n" \
    "v_mfma_f32_16x16x32_bf16 %[c42], v[216:219], a[236:239], %[c42]\n" \
    "v_mfma_f32_16x16x32_bf16 %[c03], v[220:223], a[220:223], %[c03]\n" \
    "v_mfma_f32_16x16x32_bf16 %[c13], v[220:223], a[224:227], %[c13]\n" \
    "v_mfma_f32_16x16x32_bf16 %[c23], v[220:223], a[228:231], %[c23]\n" \
    "v_mfma_f32_16x16x32_bf16 %[c33], v[220:223], a[232:235], %[c33]\n" \
    "v_mfma_f32_16x16x32_bf16 %[c43], v[220:223], a[236:239], %[c43]\n" \
    "s_branch 99f\n" \
    "80:\n" \
    "s_mov_b32 s66, 65536\n" \
    "81:\n" \
    "s_waitcnt vmcnt(0)\n" \
    "s_mov_b32 s67, 0\n" \
    "v_max3_u32 v224, v128, v129, v130\n" \
    "v_max3_u32 v224, v224, v131, v132\n" \
    "v_max3_u32 v224, v224, v133, v134\n" \
    "v_max3_u32 v224, v224, v135, v136\n" \
    "v_max3_u32 v224, v224, v137, v138\n" \
    "v_max3_u32 v224, v224, v139, v140\n" \
    "v_max3_u32 v224, v224, v141, v142\n" \
    "v_max_u32 v224, v224, v143\n" \
    "v_cmp_eq_u32 vcc, -1, v224\n" \
    "s_cbranch_vccz 820f\n" \
    "s_mov_b64 exec, vcc\n" \
    "buffer_load_dwordx4 v[128:131], %[voa], %[rs], s60 offen sc1\n" \
    "buffer_load_dwordx4 v[132:135], %[voa], %[rs], s60 offen offset:1024 sc1\n" \
    "buffer_load_dwordx4 v[136:139], %[vob], %[rs], s60 offen sc1\n" \
    "buffer_load_dwordx4 v[140:143], %[vob], %[rs], s60 offen offset:1024 sc1\n" \
    "s_mov_b64 exec, -1\n" \
    "s_mov_b32 s67, 1\n" \
    "820:\n" \
    "v_max3_u32 v224, v144, v145, v146\n" \
    "v_max3_u32 v224, v224, v147, v148\n" \
    "v_max3_u32 v224, v224, v149, v150\n" \
    "v_max3_u32 v224, v224, v151, v152\n" \
    "v_max3_u32 v224, v224, v153, v154\n" \
    "v_max3_u32 v224, v224, v155, v156\n" \
    "v_max3_u32 v224, v224, v157, v158\n" \
    "v_max_u32 v224, v224, v159\n" \
    "v_cmp_eq_u32 vcc, -1, v224\n" \
    "s_cbranch_vccz 821f\n" \
    "s_mov_b64 exec, vcc\n" \
    "buffer_load_dwordx4 v[144:147], %[voa], %[rs], s61 offen sc1\n" \
    "buffer_load_dwordx4 v[148:151], %[voa], %[rs], s61 offen offset:1024 sc1\n" \
    "buffer_load_dwordx4 v[152:155], %[vob], %[rs], s61 offen sc1\n" \
    "buffer_load_dwordx4 v[156:159], %[vob], %[rs], s61 offen offset:1024 sc1\n" \
    "s_mov_b64 exec, -1\n" \
    "s_mov_b32 s67, 1\n" \
    "821:\n" \
    "v_max3_u32 v224, v160, v161, v162\n" \
    "v_max3_u32 v224, v224, v163, v164\n" \
    "v_max3_u32 v224, v224, v165, v166\n" \
    "v_max3_u32 v224, v224, v167, v168\n" \
    "v_max3_u32 v224, v224, v169, v170\n" \
    "v_max3_u32 v224, v224, v171, v172\n" \
    "v_max3_u32 v224, v224, v173, v174\n" \
    "v_max_u32 v224, v224, v175\n" \
    "v_cmp_eq_u32 vcc, -1, v224\n" \
    "s_cbranch_vccz 822f\n" \
    "s_mov_b64 exec, vcc\n" \
    "buffer_load_dwordx4 v[160:163], %[voa], %[rs], s62 offen sc1\n" \
    "buffer_load_dwordx4 v[164:167], %[voa], %[rs], s62 offen offset:1024 sc1\n" \
    "buffer_load_dwordx4 v[168:171], %[vob], %[rs], s62 offen sc1\n" \
    "buffer_load_dwordx4 v[172:175], %[vob], %[rs], s62 offen offset:1024 sc1\n" \
    "s_mov_b64 exec, -1\n" \
    "s_mov_b32 s67, 1\n" \
    "822:\n" \
    "v_max3_u32 v224, v176, v177, v178\n" \
    "v_max3_u32 v224, v224, v179, v180\n" \
    "v_max3_u32 v224, v224, v181, v182\n" \
    "v_max3_u32 v224, v224, v183, v184\n" \
    "v_max3_u32 v224, v224, v185, v186\n" \
    "v_max3_u32 v224, v224, v187, v188\n" \
    "v_max3_u32 v224, v224, v189, v190\n" \
    "v_max_u32 v224, v224, v191\n" \
    "v_cmp_eq_u32 vcc, -1, v224\n" \
    "s_cbranch_vccz 823f\n" \
    "s_mov_b64 exec, vcc\n" \
    "buffer_load_dwordx4 v[176:179], %[voa], %[rs], s63 offen sc1\n" \
    "buffer_load_dwordx4 v[180:183], %[voa], %[rs], s63 offen offset:1024 sc1\n" \
    "buffer_load_dwordx4 v[184:187], %[vob], %[rs], s63 offen sc1\n" \
    "buffer_load_dwordx4 v[188:191], %[vob], %[rs], s63 offen offset:1024 sc1\n" \
    "s_mov_b64 exec, -1\n" \
    "s_mov_b32 s67, 1\n" \
    "823:\n" \
    "v_max3_u32 v224, v192, v193, v194\n" \
    "v_max3_u32 v224, v224, v195, v196\n" \
    "v_max3_u32 v224, v224, v197, v198\n" \
    "v_max3_u32 v224, v224, v199, v200\n" \
    "v_max3_u32 v224, v224, v201, v202\n" \
    "v_max3_u32 v224, v224, v203, v204\n" \
    "v_max3_u32 v224, v224, v205, v206\n" \
    "v_max_u32 v224, v224, v207\n" \
    "v_cmp_eq_u32 vcc, -1, v224\n" \
    "s_cbranch_vccz 824f\n" \
    "s_mov_b64 exec, vcc\n" \
    "buffer_load_dwordx4 v[192:195], %[voc], %[rs], s64 offen sc1\n" \
    "buffer_load_dwordx4 v[196:199], %[voc], %[rs], s64 offen offset:2048 sc1\n" \
    "buffer_load_dwordx4 v[200:203], %[vod], %[rs], s64 offen sc1\n" \
    "buffer_load_dwordx4 v[204:207], %[vod], %[rs], s64 offen offset:2048 sc1\n" \
    "s_mov_b64 exec, -1\n" \
    "s_mov_b32 s67, 1\n" \
    "824:\n" \
    "v_max3_u32 v224, v208, v209, v210\n" \
    "v_max3_u32 v224, v224, v211, v212\n" \
    "v_max3_u32 v224, v224, v213, v214\n" \
    "v_max3_u32 v224, v224, v215, v216\n" \
    "v_max3_u32 v224, v224, v217, v218\n" \
    "v_max3_u32 v224, v224, v219, v220\n" \
    "v_max3_u32 v224, v224, v221, v222\n" \
    "v_max_u32 v224, v224, v223\n" \
    "v_cmp_eq_u32 vcc, -1, v224\n" \
    "s_cbranch_vccz 825f\n" \
    "s_mov_b64 exec, vcc\n" \
    "buffer_load_dwordx4 v[208:211], %[voc], %[rs], s65 offen sc1\n" \
    "buffer_load_dwordx4 v[212:215], %[voc], %[rs], s65 offen offset:2048 sc1\n" \
    "buffer_load_dwordx4 v[216:219], %[vod], %[rs], s65 offen sc1\n" \
    "buffer_load_dwordx4 v[220:223], %[vod], %[rs], s65 offen offset:2048 sc1\n" \
    "s_mov_b64 exec, -1\n" \
    "s_mov_b32 s67, 1\n" \
    "825:\n" \
    "s_cmp_eq_u32 s67, 0\n" \
    "s_cbranch_scc1 83f\n" \
    "s_sub_u32 s66, s66, 1\n" \
    "s_cbranch_scc0 81b\n" \
    "83:\n" \
    "s_waitcnt vmcnt(0)\n" \
    "s_cmp_eq_u32 s68, 0\n" \
    "s_cbranch_scc1 300b\n" \
    "s_cmp_eq_u32 s68, 1\n" \
    "s_cbranch_scc1 301b\n" \
    "s_cmp_eq_u32 s68, 2\n" \
    "s_cbranch_scc1 302b\n" \
    "s_cmp_eq_u32 s68, 3\n" \
    "s_cbranch_scc1 303b\n" \
    "s_cmp_eq_u32 s68, 4\n" \
    "s_cbranch_scc1 304b\n" \
    "s_cmp_eq_u32 s68, 5\n" \
    "s_cbranch_scc1 305b\n" \
    "s_cmp_eq_u32 s68, 6\n" \
    "s_cbranch_scc1 306b\n" \
    "s_cmp_eq_u32 s68, 7\n" \
    "s_cbranch_scc1 307b\n" \
    "s_cmp_eq_u32 s68, 8\n" \
    "s_cbranch_scc1 308b\n" \
    "s_cmp_eq_u32 s68, 9\n" \
    "s_cbranch_scc1 309b\n" \
    "s_cmp_eq_u32 s68, 10\n" \
    "s_cbranch_scc1 310b\n" \
    "s_branch 311b\n" \
    "99:\n" \
    "s_nop 7\n"
#define ASM_G2_LAST \
    "s_add_u32 s60, %[hoff], 0\n" \
    "buffer_load_dwordx4 v[128:131], %[voa], %[rs], s60 offen sc1\n" \
    "buffer_load_dwordx4 v[132:135], %[voa], %[rs], s60 offen offset:1024 sc1\n" \
    "buffer_load_dwordx4 v[136:139], %[vob], %[rs], s60 offen sc1\n" \
    "buffer_load_dwordx4 v[140:143], %[vob], %[rs], s60 offen offset:1024 sc1\n" \
    "s_add_u32 s61, %[hoff], 4096\n" \
    "buffer_load_dwordx4 v[144:147], %[voa], %[rs], s61 offen sc1\n" \
    "buffer_load_dwordx4 v[148:151], %[voa], %[rs], s61 offen offset:1024 sc1\n" \
    "buffer_load_dwordx4 v[152:155], %[vob], %[rs], s61 offen sc1\n" \
    "buffer_load_dwordx4 v[156:159], %[vob], %[rs], s61 offen offset:1024 sc1\n" \
    "s_add_u32 s62, %[hoff], 8192\n" \
    "buffer_load_dwordx4 v[160:163], %[voa], %[rs], s62 offen sc1\n" \
    "buffer_load_dwordx4 v[164:167], %[voa], %[rs], s62 offen offset:1024 sc1\n" \
    "buffer_load_dwordx4 v[168:171], %[vob], %[rs], s62 offen sc1\n" \
    "buffer_load_dwordx4 v[172:175], %[vob], %[rs], s62 offen offset:1024 sc1\n" \
    "s_add_u32 s63, %[hoff], 12288\n" \
    "buffer_load_dwordx4 v[176:179], %[voa], %[rs], s63 offen sc1\n" \
    "buffer_load_dwordx4 v[180:183], %[voa], %[rs], s63 offen offset:1024 sc1\n" \
    "buffer_load_dwordx4 v[184:187], %[vob], %[rs], s63 offen sc1\n" \
    "buffer_load_dwordx4 v[188:191], %[vob], %[rs], s63 offen offset:1024 sc1\n" \
    "s_add_u32 s64, %[hoff], 16384\n" \
    "buffer_load_dwordx4 v[192:195], %[voc], %[rs], s64 offen sc1\n" \
    "buffer_load_dwordx4 v[196:199], %[voc], %[rs], s64 offen offset:2048 sc1\n" \
    "buffer_load_dwordx4 v[200:203], %[vod], %[rs], s64 offen sc1\n" \
    "buffer_load_dwordx4 v[204:207], %[vod], %[rs], s64 offen offset:2048 sc1\n" \
    "s_add_u32 s65, %[hoff], 20480\n" \
    "buffer_load_dwordx4 v[208:211], %[voc], %[rs], s65 offen sc1\n" \
    "buffer_load_dwordx4 v[212:215], %[voc], %[rs], s65 offen offset:2048 sc1\n" \
    "buffer_load_dwordx4 v[216:219], %[vod], %[rs], s65 offen sc1\n" \
    "buffer_load_dwordx4 v[220:223], %[vod], %[rs], s65 offen offset:2048 sc1\n" \
    "s_waitcnt vmcnt(20)\n" \
    "v_max3_u32 v224, v128, v129, v130\n" \
    "v_max3_u32 v224, v224, v131, v132\n" \
    "v_max3_u32 v224, v224, v133, v134\n" \
    "v_max3_u32 v224, v224, v135, v136\n" \
    "v_max3_u32 v224, v224, v137, v138\n" \
    "v_max3_u32 v224, v224, v139, v140\n" \
    "v_max3_u32 v224, v224, v141, v142\n" \
    "v_max_u32 v224, v224, v143\n" \
    "v_cmp_eq_u32 vcc, -1, v224\n" \
    "s_cbranch_vccz 300f\n" \
    "s_mov_b32 s68, 0\n" \
    "s_branch 80f\n" \
    "300:\n" \
    "s_add_u32 s60, s60, 24576\n" \
    "v_mfma_f32_16x16x32_bf16 %[c00], a[0:3], v[128:131], %[c00]\n" \
    "v_mfma_f32_16x16x32_bf16 %[c10], a[4:7], v[128:131], %[c10]\n" \
    "v_mfma_f32_16x16x32_bf16 %[c20], a[8:11], v[128:131], %[c20]\n" \
    "v_mfma_f32_16x16x32_bf16 %[c30], a[12:15], v[128:131], %[c30]\n" \
    "v_mfma_f32_16x16x32_bf16 %[c40], a[16:19], v[128:131], %[c40]\n" \
    "buffer_load_dwordx4 v[128:131], %[voa], %[rs], s60 offen sc1\n" \
    "v_mfma_f32_16x16x32_bf16 %[c01], a[0:3], v[132:135], %[c01]\n" \
    "v_mfma_f32_16x16x32_bf16 %[c11], a[4:7], v[132:135], %[c11]\n" \
    "v_mfma_f32_16x16x32_bf16 %[c21], a[8:11], v[132:135], %[c21]\n" \
    "v_mfma_f32_16x16x32_bf16 %[c31], a[12:15], v[132:135], %[c31]\n" \
    "v_mfma_f32_16x16x32_bf16 %[c41], a[16:19], v[132:135], %[c41]\n" \
    "buffer_load_dwordx4 v[132:135], %[voa], %[rs], s60 offen offset:1024 sc1\n" \
    "s_waitcnt vmcnt(18)\n" \
    "v_mfma_f32_16x16x32_bf16 %[c02], a[0:3], v[136:139], %[c02]\n" \
    "v_max3_u32 v224, v144, v145, v146\n" \
    "v_mfma_f32_16x16x32_bf16 %[c12], a[4:7], v[136:139], %[c12]\n" \
    "v_max3_u32 v224, v224, v147, v148\n" \
    "v_mfma_f32_16x16x32_bf16 %[c22], a[8:11], v[136:139], %[c22]\n" \
    "v_max3_u32 v224, v224, v149, v150\n" \
    "v_mfma_f32_16x16x32_bf16 %[c32], a[12:15], v[136:139], %[c32]\n" \
    "v_max3_u32 v224, v224, v151, v152\n" \
    "v_mfma_f32_16x16x32_bf16 %[c42], a[16:19], v[136:139], %[c42]\n" \
    "v_max3_u32 v224, v224, v153, v154\n" \
    "buffer_load_dwordx4 v[136:139], %[vob], %[rs], s60 offen sc1\n" \
    "v_mfma_f32_16x16x32_bf16 %[c03], a[0:3], v[140:143], %[c03]\n" \
    "v_max3_u32 v224, v224, v155, v156\n" \
    "v_mfma_f32_16x16x32_bf16 %[c13], a[4:7], v[140:143], %[c13]\n" \
    "v_max3_u32 v224, v224, v157, v158\n" \
    "v_mfma_f32_16x16x32_bf16 %[c23], a[8:11], v[140:143], %[c23]\n" \
    "v_max_u32 v224, v224, v159\n" \
    "v_mfma_f32_16x16x32_bf16 %[c33], a[12:15], v[140:143], %[c33]\n" \
    "v_cmp_eq_u32 vcc, -1, v224\n" \
    "v_mfma_f32_16x16x32_bf16 %[c43], a[16:19], v[140:143], %[c43]\n" \
    "buffer_load_dwordx4 v[140:143], %[vob], %[rs], s60 offen offset:1024 sc1\n" \
    "s_cbranch_vccz 301f\n" \
    "s_mov_b32 s68, 1\n" \
    "s_branch 80f\n" \
    "301:\n" \
    "s_add_u32 s61, s61, 24576\n" \
    "v_mfma_f32_16x16x32_bf16 %[c00], a[20:23], v[144:147], %[c00]\n" \
    "v_mfma_f32_16x16x32_bf16 %[c10], a[24:27], v[144:147], %[c10]\n" \
    "v_mfma_f32_16x16x32_bf16 %[c20], a[28:31], v[144:147], %[c20]\n" \
    "v_mfma_f32_16x16x32_bf16 %[c30], a[32:35], v[144:147], %[c30]\n" \
    "v_mfma_f32_16x16x32_bf16 %[c40], a[36:39], v[144:147], %[c40]\n" \
    "buffer_load_dwordx4 v[144:147], %[voa], %[rs], s61 offen sc1\n" \
    "v_mfma_f32_16x16x32_bf16 %[c01], a[20:23], v[148:151], %[c01]\n" \
    "v_mfma_f32_16x16x32_bf16 %[c11], a[24:27], v[148:151], %[c11]\n" \
    "v_mfma_f32_16x16x32_bf16 %[c21], a[28:31], v[148:151], %[c21]\n" \
    "v_mfma_f32_16x16x32_bf16 %[c31], a[32:35], v[148:151], %[c31]\n" \
    "v_mfma_f32_16x16x32_bf16 %[c41], a[36:39], v[148:151], %[c41]\n" \
    "buffer_load_dwordx4 v[148:151], %[voa], %[rs], s61 offen offset:1024 sc1\n" \
    "s_waitcnt vmcnt(18)\n" \
    "v_mfma_f32_16x16x32_bf16 %[c02], a[20:23], v[152:155], %[c02]\n" \
    "v_max3_u32 v224, v160, v161, v162\n" \
    "v_mfma_f32_16x16x32_bf16 %[c12], a[24:27], v[152:155], %[c12]\n" \
    "v_max3_u32 v224, v224, v163, v164\n" \
    "v_mfma_f32_16x16x32_bf16 %[c22], a[28:31], v[152:155], %[c22]\n" \
    "v_max3_u32 v224, v224, v165, v166\n" \
    "v_mfma_f32_16x16x32_bf16 %[c32], a[32:35], v[152:155], %[c32]\n" \
    "v_max3_u32 v224, v224, v167, v168\n" \
    "v_mfma_f32_16x16x32_bf16 %[c42], a[36:39], v[152:155], %[c42]\n" \
    "v_max3_u32 v224, v224, v169, v170\n" \
    "buffer_load_dwordx4 v[152:155], %[vob], %[rs], s61 offen sc1\n" \
    "v_mfma_f32_16x16x32_bf16 %[c03], a[20:23], v[156:159], %[c03]\n" \
    "v_max3_u32 v224, v224, v171, v172\n" \
    "v_mfma_f32_16x16x32_bf16 %[c13], a[24:27], v[156:159], %[c13]\n" \
    "v_max3_u32 v224, v224, v173, v174\n" \
    "v_mfma_f32_16x16x32_bf16 %[c23], a[28:31], v[156:159], %[c23]\n" \
    "v_max_u32 v224, v224, v175\n" \
    "v_mfma_f32_16x16x32_bf16 %[c33], a[32:35], v[156:159], %[c33]\n" \
    "v_cmp_eq_u32 vcc, -1, v224\n" \
    "v_mfma_f32_16x16x32_bf16 %[c43], a[36:39], v[156:159], %[c43]\n" \
    "buffer_load_dwordx4 v[156:159], %[vob], %[rs], s61 offen offset:1024 sc1\n" \
    "s_cbranch_vccz 302f\n" \
    "s_mov_b32 s68, 2\n" \
    "s_branch 80f\n" \
    "302:\n" \
    "s_add_u32 s62, s62, 24576\n" \
    "v_mfma_f32_16x16x32_bf16 %[c00], a[40:43], v[160:163], %[c00]\n" \
    "v_mfma_f32_16x16x32_bf16 %[c10], a[44:47], v[160:163], %[c10]\n" \
    "v_mfma_f32_16x16x32_bf16 %[c20], a[48:51], v[160:163], %[c20]\n" \
    "v_mfma_f32_16x16x32_bf16 %[c30], a[52:55], v[160:163], %[c30]\n" \
    "v_mfma_f32_16x16x32_bf16 %[c40], a[56:59], v[160:163], %[c40]\n" \
    "buffer_load_dwordx4 v[160:163], %[voa], %[rs], s62 offen sc1\n" \
    "v_mfma_f32_16x16x32_bf16 %[c01], a[40:43], v[164:167], %[c01]\n" \
    "v_mfma_f32_16x16x32_bf16 %[c11], a[44:47], v[164:167], %[c11]\n" \
    "v_mfma_f32_16x16x32_bf16 %[c21], a[48:51], v[164:167], %[c21]\n" \
    "v_mfma_f32_16x16x32_bf16 %[c31], a[52:55], v[164:167], %[c31]\n" \
    "v_mfma_f32_16x16x32_bf16 %[c41], a[56:59], v[164:167], %[c41]\n" \
    "buffer_load_dwordx4 v[164:167], %[voa], %[rs], s62 offen offset:1024 sc1\n" \
    "s_waitcnt vmcnt(18)\n" \
    "v_mfma_f32_16x16x32_bf16 %[c02], a[40:43], v[168:171], %[c02]\n" \
    "v_max3_u32 v224, v176, v177, v178\n" \
    "v_mfma_f32_16x16x32_bf16 %[c12], a[44:47], v[168:171], %[c12]\n" \
    "v_max3_u32 v224, v224, v179, v180\n" \
    "v_mfma_f32_16x16x32_bf16 %[c22], a[48:51], v[168:171], %[c22]\n" \
    "v_max3_u32 v224, v224, v181, v182\n" \
    "v_mfma_f32_16x16x32_bf16 %[c32], a[52:55], v[168:171], %[c32]\n" \
    "v_max3_u32 v224, v224, v183, v184\n" \
    "v_mfma_f32_16x16x32_bf16 %[c42], a[56:59], v[168:171], %[c42]\n" \
    "v_max3_u32 v224, v224, v185, v186\n" \
    "buffer_load_dwordx4 v[168:171], %[vob], %[rs], s62 offen sc1\n" \
    "v_mfma_f32_16x16x32_bf16 %[c03], a[40:43], v[172:175], %[c03]\n" \
    "v_max3_u32 v224, v224, v187, v188\n" \
    "v_mfma_f32_16x16x32_bf16 %[c13], a[44:47], v[172:175], %[c13]\n" \
    "v_max3_u32 v224, v224, v189, v190\n" \
    "v_mfma_f32_16x16x32_bf16 %[c23], a[48:51], v[172:175], %[c23]\n" \
    "v_max_u32 v224, v224, v191\n" \
    "v_mfma_f32_16x16x32_bf16 %[c33], a[52:55], v[172:175], %[c33]\n" \
    "v_cmp_eq_u32 vcc, -1, v224\n" \
    "v_mfma_f32_16x16x32_bf16 %[c43], a[56:59], v[172:175], %[c43]\n" \
    "buffer_load_dwordx4 v[172:175], %[vob], %[rs], s62 offen offset:1024 sc1\n" \
    "s_cbranch_vccz 303f\n" \
    "s_mov_b32 s68, 3\n" \
    "s_branch 80f\n" \
    "303:\n" \
    "s_add_u32 s63, s63, 24576\n" \
    "v_mfma_f32_16x16x32_bf16 %[c00], a[60:63], v[176:179], %[c00]\n" \
    "v_mfma_f32_16x16x32_bf16 %[c10], a[64:67], v[176:179], %[c10]\n" \
    "v_mfma_f32_16x16x32_bf16 %[c20], a[68:71], v[176:179], %[c20]\n" \
    "v_mfma_f32_16x16x32_bf16 %[c30], a[72:75], v[176:179], %[c30]\n" \
    "v_mfma_f32_16x16x32_bf16 %[c40], a[76:79], v[176:179], %[c40]\n" \
    "buffer_load_dwordx4 v[176:179], %[voa], %[rs], s63 offen sc1\n" \
    "v_mfma_f32_16x16x32_bf16 %[c01], a[60:63], v[180:183], %[c01]\n" \
    "v_mfma_f32_16x16x32_bf16 %[c11], a[64:67], v[180:183], %[c11]\n" \
    "v_mfma_f32_16x16x32_bf16 %[c21], a[68:71], v[180:183], %[c21]\n" \
    "v_mfma_f32_16x16x32_bf16 %[c31], a[72:75], v[180:183], %[c31]\n" \
    "v_mfma_f32_16x16x32_bf16 %[c41], a[76:79], v[180:183], %[c41]\n" \
    "buffer_load_dwordx4 v[180:183], %[voa], %[rs], s63 offen offset:1024 sc1\n" \
    "s_waitcnt vmcnt(18)\n" \
    "v_mfma_f32_16x16x32_bf16 %[c02], a[60:63], v[184:187], %[c02]\n" \
    "v_max3_u32 v224, v192, v193, v194\n" \
    "v_mfma_f32_16x16x32_bf16 %[c12], a[64:67], v[184:187], %[c12]\n" \
    "v_max3_u32 v224, v224, v195, v196\n" \
    "v_mfma_f32_16x16x32_bf16 %[c22], a[68:71], v[184:187], %[c22]\n" \
    "v_max3_u32 v224, v224, v197, v198\n" \
    "v_mfma_f32_16x16x32_bf16 %[c32], a[72:75], v[184:187], %[c32]\n" \
    "v_max3_u32 v224, v224, v199, v200\n" \
    "v_mfma_f32_16x16x32_bf16 %[c42], a[76:79], v[184:187], %[c42]\n" \
    "v_max3_u32 v224, v224, v201, v202\n" \
    "buffer_load_dwordx4 v[184:187], %[vob], %[rs], s63 offen sc1\n" \
    "v_mfma_f32_16x16x32_bf16 %[c03], a[60:63], v[188:191], %[c03]\n" \
    "v_max3_u32 v224, v224, v203, v204\n" \
    "v_mfma_f32_16x16x32_bf16 %[c13], a[64:67], v[188:191], %[c13]\n" \
    "v_max3_u32 v224, v224, v205, v206\n" \
    "v_mfma_f32_16x16x32_bf16 %[c23], a[68:71], v[188:191], %[c23]\n" \
    "v_max_u32 v224, v224, v207\n" \
    "v_mfma_f32_16x16x32_bf16 %[c33], a[72:75], v[188:191], %[c33]\n" \
    "v_cmp_eq_u32 vcc, -1, v224\n" \
    "v_mfma_f32_16x16x32_bf16 %[c43], a[76:79], v[188:191], %[c43]\n" \
    "buffer_load_dwordx4 v[188:191], %[vob], %[rs], s63 offen offset:1024 sc1\n" \
    "s_cbranch_vccz 304f\n" \
    "s_mov_b32 s68, 4\n" \
    "s_branch 80f\n" \
    "304:\n" \
    "v_swap_b32 v194, v196\n" \
    "v_swap_b32 v195, v197\n" \
    "v_swap_b32 v202, v204\n" \
    "v_swap_b32 v203, v205\n" \
    "s_nop 1\n" \
    "s_add_u32 s64, s64, 24576\n" \
    "v_mfma_f32_16x16x32_bf16 %[c00], a[80:83], v[192:195], %[c00]\n" \
    "v_mfma_f32_16x16x32_bf16 %[c10], a[84:87], v[192:195], %[c10]\n" \
    "v_mfma_f32_16x16x32_bf16 %[c20], a[88:91], v[192:195], %[c20]\n" \
    "v_mfma_f32_16x16x32_bf16 %[c30], a[92:95], v[192:195], %[c30]\n" \
    "v_mfma_f32_16x16x32_bf16 %[c40], a[96:99], v[192:195], %[c40]\n" \
    "buffer_load_dwordx4 v[192:195], %[voc], %[rs], s64 offen sc1\n" \
    "v_mfma_f32_16x16x32_bf16 %[c01], a[80:83], v[196:199], %[c01]\n" \
    "v_mfma_f32_16x16x32_bf16 %[c11], a[84:87], v[196:199], %[c11]\n" \
    "v_mfma_f32_16x16x32_bf16 %[c21], a[88:91], v[196:199], %[c21]\n" \
    "v_mfma_f32_16x16x32_bf16 %[c31], a[92:95], v[196:199], %[c31]\n" \
    "v_mfma_f32_16x16x32_bf16 %[c41], a[96:99], v[196:199], %[c41]\n" \
    "buffer_load_dwordx4 v[196:199], %[voc], %[rs], s64 offen offset:2048 sc1\n" \
    "s_waitcnt vmcnt(18)\n" \
    "v_mfma_f32_16x16x32_bf16 %[c02], a[80:83], v[200:203], %[c02]\n" \
    "v_max3_u32 v224, v208, v209, v210\n" \
    "v_mfma_f32_16x16x32_bf16 %[c12], a[84:87], v[200:203], %[c12]\n" \
    "v_max3_u32 v224, v224, v211, v212\n" \
    "v_mfma_f32_16x16x32_bf16 %[c22], a[88:91], v[200:203], %[c22]\n" \
    "v_max3_u32 v224, v224, v213, v214\n" \
    "v_mfma_f32_16x16x32_bf16 %[c32], a[92:95], v[200:203], %[c32]\n" \
    "v_max3_u32 v224, v224, v215, v216\n" \
    "v_mfma_f32_16x16x32_bf16 %[c42], a[96:99], v[200:203], %[c42]\n" \
    "v_max3_u32 v224, v224, v217, v218\n" \
    "buffer_load_dwordx4 v[200:203], %[vod], %[rs], s64 offen sc1\n" \
    "v_mfma_f32_16x16x32_bf16 %[c03], a[80:83], v[204:207], %[c03]\n" \
    "v_max3_u32 v224, v224, v219, v220\n" \
    "v_mfma_f32_16x16x32_bf16 %[c13], a[84:87], v[204:207], %[c13]\n" \
    "v_max3_u32 v224, v224, v221, v222\n" \
    "v_mfma_f32_16x16x32_bf16 %[c23], a[88:91], v[204:207], %[c23]\n" \
    "v_max_u32 v224, v224, v223\n" \
    "v_mfma_f32_16x16x32_bf16 %[c33], a[92:95], v[204:207], %[c33]\n" \
    "v_cmp_eq_u32 vcc, -1, v224\n" \
    "v_mfma_f32_16x16x32_bf16 %[c43], a[96:99], v[204:207], %[c43]\n" \
    "buffer_load_dwordx4 v[204:207], %[vod], %[rs], s64 offen offset:2048 sc1\n" \
    "s_cbranch_vccz 305f\n" \
    "s_mov_b32 s68, 5\n" \
    "s_branch 80f\n" \
    "305:\n" \
    "v_swap_b32 v210, v212\n" \
    "v_swap_b32 v211, v213\n" \
    "v_swap_b32 v218, v220\n" \
    "v_swap_b32 v219, v221\n" \
    "s_nop 1\n" \
    "s_add_u32 s65, s65, 24576\n" \
    "v_mfma_f32_16x16x32_bf16 %[c00], a[100:103], v[208:211], %[c00]\n" \
    "v_mfma_f32_16x16x32_bf16 %[c10], a[104:107], v[208:211], %[c10]\n" \
    "v_mfma_f32_16x16x32_bf16 %[c20], a[108:111], v[208:211], %[c20]\n" \
    "v_mfma_f32_16x16x32_bf16 %[c30], a[112:115], v[208:211], %[c30]\n" \
    "v_mfma_f32_16x16x32_bf16 %[c40], a[116:119], v[208:211], %[c40]\n" \
    "buffer_load_dwordx4 v[208:211], %[voc], %[rs], s65 offen sc1\n" \
    "v_mfma_f32_16x16x32_bf16 %[c01], a[100:103], v[212:215], %[c01]\n" \
    "v_mfma_f32_16x16x32_bf16 %[c11], a[104:107], v[212:215], %[c11]\n" \
    "v_mfma_f32_16x16x32_bf16 %[c21], a[108:111], v[212:215], %[c21]\n" \
    "v_mfma_f32_16x16x32_bf16 %[c31], a[112:115], v[212:215], %[c31]\n" \
    "v_mfma_f32_16x16x32_bf16 %[c41], a[116:119], v[212:215], %[c41]\n" \
    "buffer_load_dwordx4 v[212:215], %[voc], %[rs], s65 offen offset:2048 sc1\n" \
    "s_waitcnt vmcnt(18)\n" \
    "v_mfma_f32_16x16x32_bf16 %[c02], a[100:103], v[216:219], %[c02]\n" \
    "v_max3_u32 v224, v128, v129, v130\n" \
    "v_mfma_f32_16x16x32_bf16 %[c12], a[104:107], v[216:219], %[c12]\n" \
    "v_max3_u32 v224, v224, v131, v132\n" \
    "v_mfma_f32_16x16x32_bf16 %[c22], a[108:111], v[216:219], %[c22]\n" \
    "v_max3_u32 v224, v224, v133, v134\n" \
    "v_mfma_f32_16x16x32_bf16 %[c32], a[112:115], v[216:219], %[c32]\n" \
    "v_max3_u32 v224, v224, v135, v136\n" \
    "v_mfma_f32_16x16x32_bf16 %[c42], a[116:119], v[216:219], %[c42]\n" \
    "v_max3_u32 v224, v224, v137, v138\n" \
    "buffer_load_dwordx4 v[216:219], %[vod], %[rs], s65 offen sc1\n" \
    "v_mfma_f32_16x16x32_bf16 %[c03], a[100:103], v[220:223], %[c03]\n" \
    "v_max3_u32 v224, v224, v139, v140\n" \
    "v_mfma_f32_16x16x32_bf16 %[c13], a[104:107], v[220:223], %[c13]\n" \
    "v_max3_u32 v224, v224, v141, v142\n" \
    "v_mfma_f32_16x16x32_bf16 %[c23], a[108:111], v[220:223], %[c23]\n" \
    "v_max_u32 v224, v224, v143\n" \
    "v_mfma_f32_16x16x32_bf16 %[c33], a[112:115], v[220:223], %[c33]\n" \
    "v_cmp_eq_u32 vcc, -1, v224\n" \
    "v_mfma_f32_16x16x32_bf16 %[c43], a[116:119], v[220:223], %[c43]\n" \
    "buffer_load_dwordx4 v[220:223], %[vod], %[rs], s65 offen offset:2048 sc1\n" \
    "s_cbranch_vccz 306f\n" \
    "s_mov_b32 s68, 6\n" \
    "s_branch 80f\n" \
    "306:\n" \
    "v_mfma_f32_16x16x32_bf16 %[c00], a[120:123], v[128:131], %[c00]\n" \
    "v_mfma_f32_16x16x32_bf16 %[c10], a[124:127], v[128:131], %[c10]\n" \
    "v_mfma_f32_16x16x32_bf16 %[c20], a[128:131], v[128:131], %[c20]\n" \
    "v_mfma_f32_16x16x32_bf16 %[c30], a[132:135], v[128:131], %[c30]\n" \
    "v_mfma_f32_16x16x32_bf16 %[c40], a[136:139], v[128:131], %[c40]\n" \
    "v_mfma_f32_16x16x32_bf16 %[c01], a[120:123], v[132:135], %[c01]\n" \
    "v_mfma_f32_16x16x32_bf16 %[c11], a[124:127], v[132:135], %[c11]\n" \
    "v_mfma_f32_16x16x32_bf16 %[c21], a[128:131], v[132:135], %[c21]\n" \
    "v_mfma_f32_16x16x32_bf16 %[c31], a[132:135], v[132:135], %[c31]\n" \
    "v_mfma_f32_16x16x32_bf16 %[c41], a[136:139], v[132:135], %[c41]\n" \
    "s_waitcnt vmcnt(16)\n" \
    "v_mfma_f32_16x16x32_bf16 %[c02], a[120:123], v[136:139], %[c02]\n" \
    "v_max3_u32 v224, v144, v145, v146\n" \
    "v_mfma_f32_16x16x32_bf16 %[c12], a[124:127], v[136:139], %[c12]\n" \
    "v_max3_u32 v224, v224, v147, v148\n" \
    "v_mfma_f32_16x16x32_bf16 %[c22], a[128:131], v[136:139], %[c22]\n" \
    "v_max3_u32 v224, v224, v149, v150\n" \
    "v_mfma_f32_16x16x32_bf16 %[c32], a[132:135], v[136:139], %[c32]\n" \
    "v_max3_u32 v224, v224, v151, v152\n" \
    "v_mfma_f32_16x16x32_bf16 %[c42], a[136:139], v[136:139], %[c42]\n" \
    "v_max3_u32 v224, v224, v153, v154\n" \
    "v_mfma_f32_16x16x32_bf16 %[c03], a[120:123], v[140:143], %[c03]\n" \
    "v_max3_u32 v224, v224, v155, v156\n" \
    "v_mfma_f32_16x16x32_bf16 %[c13], a[124:127], v[140:143], %[c13]\n" \
    "v_max3_u32 v224, v224, v157, v158\n" \
    "v_mfma_f32_16x16x32_bf16 %[c23], a[128:131], v[140:143], %[c23]\n" \
    "v_max_u32 v224, v224, v159\n" \
    "v_mfma_f32_16x16x32_bf16 %[c33], a[132:135], v[140:143], %[c33]\n" \
    "v_cmp_eq_u32 vcc, -1, v224\n" \
    "v_mfma_f32_16x16x32_bf16 %[c43], a[136:139], v[140:143], %[c43]\n" \
    "s_cbranch_vccz 307f\n" \
    "s_mov_b32 s68, 7\n" \
    "s_branch 80f\n" \
    "307:\n" \
    "v_mfma_f32_16x16x32_bf16 %[c00], a[140:143], v[144:147], %[c00]\n" \
    "v_mfma_f32_16x16x32_bf16 %[c10], a[144:147], v[144:147], %[c10]\n" \
    "v_mfma_f32_16x16x32_bf16 %[c20], a[148:151], v[144:147], %[c20]\n" \
    "v_mfma_f32_16x16x32_bf16 %[c30], a[152:155], v[144:147], %[c30]\n" \
    "v_mfma_f32_16x16x32_bf16 %[c40], a[156:159], v[144:147], %[c40]\n" \
    "v_mfma_f32_16x16x32_bf16 %[c01], a[140:143], v[148:151], %[c01]\n" \
    "v_mfma_f32_16x16x32_bf16 %[c11], a[144:147], v[148:151], %[c11]\n" \
    "v_mfma_f32_16x16x32_bf16 %[c21], a[148:151], v[148:151], %[c21]\n" \
    "v_mfma_f32_16x16x32_bf16 %[c31], a[152:155], v[148:151], %[c31]\n" \
    "v_mfma_f32_16x16x32_bf16 %[c41], a[156:159], v[148:151], %[c41]\n" \
    "s_waitcnt vmcnt(12)\n" \
    "v_mfma_f32_16x16x32_bf16 %[c02], a[140:143], v[152:155], %[c02]\n" \
    "v_max3_u32 v224, v160, v161, v162\n" \
    "v_mfma_f32_16x16x32_bf16 %[c12], a[144:147], v[152:155], %[c12]\n" \
    "v_max3_u32 v224, v224, v163, v164\n" \
    "v_mfma_f32_16x16x32_bf16 %[c22], a[148:151], v[152:155], %[c22]\n" \
    "v_max3_u32 v224, v224, v165, v166\n" \
    "v_mfma_f32_16x16x32_bf16 %[c32], a[152:155], v[152:155], %[c32]\n" \
    "v_max3_u32 v224, v224, v167, v168\n" \
    "v_mfma_f32_16x16x32_bf16 %[c42], a[156:159], v[152:155], %[c42]\n" \
    "v_max3_u32 v224, v224, v169, v170\n" \
    "v_mfma_f32_16x16x32_bf16 %[c03], a[140:143], v[156:159], %[c03]\n" \
    "v_max3_u32 v224, v224, v171, v172\n" \
    "v_mfma_f32_16x16x32_bf16 %[c13], a[144:147], v[156:159], %[c13]\n" \
    "v_max3_u32 v224, v224, v173, v174\n" \
    "v_mfma_f32_16x16x32_bf16 %[c23], a[148:151], v[156:159], %[c23]\n" \
    "v_max_u32 v224, v224, v175\n" \
    "v_mfma_f32_16x16x32_bf16 %[c33], a[152:155], v[156:159], %[c33]\n" \
    "v_cmp_eq_u32 vcc, -1, v224\n" \
    "v_mfma_f32_16x16x32_bf16 %[c43], a[156:159], v[156:159], %[c43]\n" \
    "s_cbranch_vccz 308f\n" \
    "s_mov_b32 s68, 8\n" \
    "s_branch 80f\n" \
    "308:\n" \
    "v_mfma_f32_16x16x32_bf16 %[c00], a[160:163], v[160:163], %[c00]\n" \
    "v_mfma_f32_16x16x32_bf16 %[c10], a[164:167], v[160:163], %[c10]\n" \
    "v_mfma_f32_16x16x32_bf16 %[c20], a[168:171], v[160:163], %[c20]\n" \
    "v_mfma_f32_16x16x32_bf16 %[c30], a[172:175], v[160:163], %[c30]\n" \
    "v_mfma_f32_16x16x32_bf16 %[c40], a[176:179], v[160:163], %[c40]\n" \
    "v_mfma_f32_16x16x32_bf16 %[c01], a[160:163], v[164:167], %[c01]\n" \
    "v_mfma_f32_16x16x32_bf16 %[c11], a[164:167], v[164:167], %[c11]\n" \
    "v_mfma_f32_16x16x32_bf16 %[c21], a[168:171], v[164:167], %[c21]\n" \
    "v_mfma_f32_16x16x32_bf16 %[c31], a[172:175], v[164:167], %[c31]\n" \
    "v_mfma_f32_16x16x32_bf16 %[c41], a[176:179], v[164:167], %[c41]\n" \
    "s_waitcnt vmcnt(8)\n" \
    "v_mfma_f32_16x16x32_bf16 %[c02], a[160:163], v[168:171], %[c02]\n" \
    "v_max3_u32 v224, v176, v177, v178\n" \
    "v_mfma_f32_16x16x32_bf16 %[c12], a[164:167], v[168:171], %[c12]\n" \
    "v_max3_u32 v224, v224, v179, v180\n" \
    "v_mfma_f32_16x16x32_bf16 %[c22], a[168:171], v[168:171], %[c22]\n" \
    "v_max3_u32 v224, v224, v181, v182\n" \
    "v_mfma_f32_16x16x32_bf16 %[c32], a[172:175], v[168:171], %[c32]\n" \
    "v_max3_u32 v224, v224, v183, v184\n" \
    "v_mfma_f32_16x16x32_bf16 %[c42], a[176:179], v[168:171], %[c42]\n" \
    "v_max3_u32 v224, v224, v185, v186\n" \
    "v_mfma_f32_16x16x32_bf16 %[c03], a[160:163], v[172:175], %[c03]\n" \
    "v_max3_u32 v224, v224, v187, v188\n" \
    "v_mfma_f32_16x16x32_bf16 %[c13], a[164:167], v[172:175], %[c13]\n" \
    "v_max3_u32 v224, v224, v189, v190\n" \
    "v_mfma_f32_16x16x32_bf16 %[c23], a[168:171], v[172:175], %[c23]\n" \
    "v_max_u32 v224, v224, v191\n" \
    "v_mfma_f32_16x16x32_bf16 %[c33], a[172:175], v[172:175], %[c33]\n" \
    "v_cmp_eq_u32 vcc, -1, v224\n" \
    "v_mfma_f32_16x16x32_bf16 %[c43], a[176:179], v[172:175], %[c43]\n" \
    "s_cbranch_vccz 309f\n" \
    "s_mov_b32 s68, 9\n" \
    "s_branch 80f\n" \
    "309:\n" \
    "v_mfma_f32_16x16x32_bf16 %[c00], a[180:183], v[176:179], %[c00]\n" \
    "v_mfma_f32_16x16x32_bf16 %[c10], a[184:187], v[176:179], %[c10]\n" \
    "v_mfma_f32_16x16x32_bf16 %[c20], a[188:191], v[176:179], %[c20]\n" \
    "v_mfma_f32_16x16x32_bf16 %[c30], a[192:195], v[176:179], %[c30]\n" \
    "v_mfma_f32_16x16x32_bf16 %[c40], a[196:199], v[176:179], %[c40]\n" \
    "v_mfma_f32_16x16x32_bf16 %[c01], a[180:183], v[180:183], %[c01]\n" \
    "v_mfma_f32_16x16x32_bf16 %[c11], a[184:187], v[180:183], %[c11]\n" \
    "v_mfma_f32_16x16x32_bf16 %[c21], a[188:191], v[180:183], %[c21]\n" \
    "v_mfma_f32_16x16x32_bf16 %[c31], a[192:195], v[180:183], %[c31]\n" \
    "v_mfma_f32_16x16x32_bf16 %[c41], a[196:199], v[180:183], %[c41]\n" \
    "s_waitcnt vmcnt(4)\n" \
    "v_mfma_f32_16x16x32_bf16 %[c02], a[180:183], v[184:187], %[c02]\n" \
    "v_max3_u32 v224, v192, v193, v194\n" \
    "v_mfma_f32_16x16x32_bf16 %[c12], a[184:187], v[184:187], %[c12]\n" \
    "v_max3_u32 v224, v224, v195, v196\n" \
    "v_mfma_f32_16x16x32_bf16 %[c22], a[188:191], v[184:187], %[c22]\n" \
    "v_max3_u32 v224, v224, v197, v198\n" \
    "v_mfma_f32_16x16x32_bf16 %[c32], a[192:195], v[184:187], %[c32]\n" \
    "v_max3_u32 v224, v224, v199, v200\n" \
    "v_mfma_f32_16x16x32_bf16 %[c42], a[196:199], v[184:187], %[c42]\n" \
    "v_max3_u32 v224, v224, v201, v202\n" \
    "v_mfma_f32_16x16x32_bf16 %[c03], a[180:183], v[188:191], %[c03]\n" \
    "v_max3_u32 v224, v224, v203, v204\n" \
    "v_mfma_f32_16x16x32_bf16 %[c13], a[184:187], v[188:191], %[c13]\n" \
    "v_max3_u32 v224, v224, v205, v206\n" \
    "v_mfma_f32_16x16x32_bf16 %[c23], a[188:191], v[188:191], %[c23]\n" \
    "v_max_u32 v224, v224, v207\n" \
    "v_mfma_f32_16x16x32_bf16 %[c33], a[192:195], v[188:191], %[c33]\n" \
    "v_cmp_eq_u32 vcc, -1, v224\n" \
    "v_mfma_f32_16x16x32_bf16 %[c43], a[196:199], v[188:191], %[c43]\n" \
    "s_cbranch_vccz 310f\n" \
    "s_mov_b32 s68, 10\n" \
    "s_branch 80f\n" \
    "310:\n" \
    "v_swap_b32 v194, v196\n" \
    "v_swap_b32 v195, v197\n" \
    "v_swap_b32 v202, v204\n" \
    "v_swap_b32 v203, v205\n" \
    "s_nop 1\n" \
    "v_mfma_f32_16x16x32_bf16 %[c00], a[200:203], v[192:195], %[c00]\n" \
    "v_mfma_f32_16x16x32_bf16 %[c10], a[204:207], v[192:195], %[c10]\n" \
    "v_mfma_f32_16x16x32_bf16 %[c20], a[208:211], v[192:195], %[c20]\n" \
    "v_mfma_f32_16x16x32_bf16 %[c30], a[212:215], v[192:195], %[c30]\n" \
    "v_mfma_f32_16x16x32_bf16 %[c40], a[216:219], v[192:195], %[c40]\n" \
    "v_mfma_f32_16x16x32_bf16 %[c01], a[200:203], v[196:199], %[c01]\n" \
    "v_mfma_f32_16x16x32_bf16 %[c11], a[204:207], v[196:199], %[c11]\n" \
    "v_mfma_f32_16x16x32_bf16 %[c21], a[208:211], v[196:199], %[c21]\n" \
    "v_mfma_f32_16x16x32_bf16 %[c31], a[212:215], v[196:199], %[c31]\n" \
    "v_mfma_f32_16x16x32_bf16 %[c41], a[216:219], v[196:199], %[c41]\n" \
    "s_waitcnt vmcnt(0)\n" \
    "v_mfma_f32_16x16x32_bf16 %[c02], a[200:203], v[200:203], %[c02]\n" \
    "v_max3_u32 v224, v208, v209, v210\n" \
    "v_mfma_f32_16x16x32_bf16 %[c12], a[204:207], v[200:203], %[c12]\n" \
    "v_max3_u32 v224, v224, v211, v212\n" \
    "v_mfma_f32_16x16x32_bf16 %[c22], a[208:211], v[200:203], %[c22]\n" \
    "v_max3_u32 v224, v224, v213, v214\n" \
    "v_mfma_f32_16x16x32_bf16 %[c32], a[212:215], v[200:203], %[c32]\n" \
    "v_max3_u32 v224, v224, v215, v216\n" \
    "v_mfma_f32_16x16x32_bf16 %[c42], a[216:219], v[200:203], %[c42]\n" \
    "v_max3_u32 v224, v224, v217, v218\n" \
    "v_mfma_f32_16x16x32_bf16 %[c03], a[200:203], v[204:207], %[c03]\n" \
    "v_max3_u32 v224, v224, v219, v220\n" \
    "v_mfma_f32_16x16x32_bf16 %[c13], a[204:207], v[204:207], %[c13]\n" \
    "v_max3_u32 v224, v224, v221, v222\n" \
    "v_mfma_f32_16x16x32_bf16 %[c23], a[208:211], v[204:207], %[c23]\n" \
    "v_max_u32 v224, v224, v223\n" \
    "v_mfma_f32_16x16x32_bf16 %[c33], a[212:215], v[204:207], %[c33]\n" \
    "v_cmp_eq_u32 vcc, -1, v224\n" \
    "v_mfma_f32_16x16x32_bf16 %[c43], a[216:219], v[204:207], %[c43]\n" \
    "s_cbranch_vccz 311f\n" \
    "s_mov_b32 s68, 11\n" \
    "s_branch 80f\n" \
    "311:\n" \
    "v_swap_b32 v210, v212\n" \
    "v_swap_b32 v211, v213\n" \
    "v_swap_b32 v218, v220\n" \
    "v_swap_b32 v219, v221\n" \
    "s_nop 1\n" \
    "v_mfma_f32_16x16x32_bf16 %[c00], a[220:223], v[208:211], %[c00]\n" \
    "v_mfma_f32_16x16x32_bf16 %[c10], a[224:227], v[208:211], %[c10]\n" \
    "v_mfma_f32_16x16x32_bf16 %[c20], a[228:231], v[208:211], %[c20]\n" \
    "v_mfma_f32_16x16x32_bf16 %[c30], a[232:235], v[208:211], %[c30]\n" \
    "v_mfma_f32_16x16x32_bf16 %[c40], a[236:239], v[208:211], %[c40]\n" \
    "v_mfma_f32_16x16x32_bf16 %[c01], a[220:223], v[212:215], %[c01]\n" \
    "v_mfma_f32_16x16x32_bf16 %[c11], a[224:227], v[212:215], %[c11]\n" \
    "v_mfma_f32_16x16x32_bf16 %[c21], a[228:231], v[212:215], %[c21]\n" \
    "v_mfma_f32_16x16x32_bf16 %[c31], a[232:235], v[212:215], %[c31]\n" \
    "v_mfma_f32_16x16x32_bf16 %[c41], a[236:239], v[212:215], %[c41]\n" \
    "v_mfma_f32_16x16x32_bf16 %[c02], a[220:223], v[216:219], %[c02]\n" \
    "v_mfma_f32_16x16x32_bf16 %[c12], a[224:227], v[216:219], %[c12]\n" \
    "v_mfma_f32_16x16x32_bf16 %[c22], a[228:231], v[216:219], %[c22]\n" \
    "v_mfma_f32_16x16x32_bf16 %[c32], a[232:235], v[216:219], %[c32]\n" \
    "v_mfma_f32_16x16x32_bf16 %[c42], a[236:239], v[216:219], %[c42]\n" \
    "v_mfma_f32_16x16x32_bf16 %[c03], a[220:223], v[220:223], %[c03]\n" \
    "v_mfma_f32_16x16x32_bf16 %[c13], a[224:227], v[220:223], %[c13]\n" \
    "v_mfma_f32_16x16x32_bf16 %[c23], a[228:231], v[220:223], %[c23]\n" \
    "v_mfma_f32_16x16x32_bf16 %[c33], a[232:235], v[220:223], %[c33]\n" \
    "v_mfma_f32_16x16x32_bf16 %[c43], a[236:239], v[220:223], %[c43]\n" \
    "s_branch 99f\n" \
    "80:\n" \
    "s_mov_b32 s66, 65536\n" \
    "81:\n" \
    "s_waitcnt vmcnt(0)\n" \
    "s_mov_b32 s67, 0\n" \
    "v_max3_u32 v224, v128, v129, v130\n" \
    "v_max3_u32 v224, v224, v131, v132\n" \
    "v_max3_u32 v224, v224, v133, v134\n" \
    "v_max3_u32 v224, v224, v135, v136\n" \
    "v_max3_u32 v224, v224, v137, v138\n" \
    "v_max3_u32 v224, v224, v139, v140\n" \
    "v_max3_u32 v224, v224, v141, v142\n" \
    "v_max_u32 v224, v224, v143\n" \
    "v_cmp_eq_u32 vcc, -1, v224\n" \
    "s_cbranch_vccz 820f\n" \
    "s_mov_b64 exec, vcc\n" \
    "buffer_load_dwordx4 v[128:131], %[voa], %[rs], s60 offen sc1\n" \
    "buffer_load_dwordx4 v[132:135], %[voa], %[rs], s60 offen offset:1024 sc1\n" \
    "buffer_load_dwordx4 v[136:139], %[vob], %[rs], s60 offen sc1\n" \
    "buffer_load_dwordx4 v[140:143], %[vob], %[rs], s60 offen offset:1024 sc1\n" \
    "s_mov_b64 exec, -1\n" \
    "s_mov_b32 s67, 1\n" \
    "820:\n" \
    "v_max3_u32 v224, v144, v145, v146\n" \
    "v_max3_u32 v224, v224, v147, v148\n" \
    "v_max3_u32 v224, v224, v149, v150\n" \
    "v_max3_u32 v224, v224, v151, v152\n" \
    "v_max3_u32 v224, v224, v153, v154\n" \
    "v_max3_u32 v224, v224, v155, v156\n" \
    "v_max3_u32 v224, v224, v157, v158\n" \
    "v_max_u32 v224, v224, v159\n" \
    "v_cmp_eq_u32 vcc, -1, v224\n" \
    "s_cbranch_vccz 821f\n" \
    "s_mov_b64 exec, vcc\n" \
    "buffer_load_dwordx4 v[144:147], %[voa], %[rs], s61 offen sc1\n" \
    "buffer_load_dwordx4 v[148:151], %[voa], %[rs], s61 offen offset:1024 sc1\n" \
    "buffer_load_dwordx4 v[152:155], %[vob], %[rs], s61 offen sc1\n" \
    "buffer_load_dwordx4 v[156:159], %[vob], %[rs], s61 offen offset:1024 sc1\n" \
    "s_mov_b64 exec, -1\n" \
    "s_mov_b32 s67, 1\n" \
    "821:\n" \
    "v_max3_u32 v224, v160, v161, v162\n" \
    "v_max3_u32 v224, v224, v163, v164\n" \
    "v_max3_u32 v224, v224, v165, v166\n" \
    "v_max3_u32 v224, v224, v167, v168\n" \
    "v_max3_u32 v224, v224, v169, v170\n" \
    "v_max3_u32 v224, v224, v171, v172\n" \
    "v_max3_u32 v224, v224, v173, v174\n" \
    "v_max_u32 v224, v224, v175\n" \
    "v_cmp_eq_u32 vcc, -1, v224\n" \
    "s_cbranch_vccz 822f\n" \
    "s_mov_b64 exec, vcc\n" \
    "buffer_load_dwordx4 v[160:163], %[voa], %[rs], s62 offen sc1\n" \
    "buffer_load_dwordx4 v[164:167], %[voa], %[rs], s62 offen offset:1024 sc1\n" \
    "buffer_load_dwordx4 v[168:171], %[vob], %[rs], s62 offen sc1\n" \
    "buffer_load_dwordx4 v[172:175], %[vob], %[rs], s62 offen offset:1024 sc1\n" \
    "s_mov_b64 exec, -1\n" \
    "s_mov_b32 s67, 1\n" \
    "822:\n" \
    "v_max3_u32 v224, v176, v177, v178\n" \
    "v_max3_u32 v224, v224, v179, v180\n" \
    "v_max3_u32 v224, v224, v181, v182\n" \
    "v_max3_u32 v224, v224, v183, v184\n" \
    "v_max3_u32 v224, v224, v185, v186\n" \
    "v_max3_u32 v224, v224, v187, v188\n" \
    "v_max3_u32 v224, v224, v189, v190\n" \
    "v_max_u32 v224, v224, v191\n" \
    "v_cmp_eq_u32 vcc, -1, v224\n" \
    "s_cbranch_vccz 823f\n" \
    "s_mov_b64 exec, vcc\n" \
    "buffer_load_dwordx4 v[176:179], %[voa], %[rs], s63 offen sc1\n" \
    "buffer_load_dwordx4 v[180:183], %[voa], %[rs], s63 offen offset:1024 sc1\n" \
    "buffer_load_dwordx4 v[184:187], %[vob], %[rs], s63 offen sc1\n" \
    "buffer_load_dwordx4 v[188:191], %[vob], %[rs], s63 offen offset:1024 sc1\n" \
    "s_mov_b64 exec, -1\n" \
    "s_mov_b32 s67, 1\n" \
    "823:\n" \
    "v_max3_u32 v224, v192, v193, v194\n" \
    "v_max3_u32 v224, v224, v195, v196\n" \
    "v_max3_u32 v224, v224, v197, v198\n" \
    "v_max3_u32 v224, v224, v199, v200\n" \
    "v_max3_u32 v224, v224, v201, v202\n" \
    "v_max3_u32 v224, v224, v203, v204\n" \
    "v_max3_u32 v224, v224, v205, v206\n" \
    "v_max_u32 v224, v224, v207\n" \
    "v_cmp_eq_u32 vcc, -1, v224\n" \
    "s_cbranch_vccz 824f\n" \
    "s_mov_b64 exec, vcc\n" \
    "buffer_load_dwordx4 v[192:195], %[voc], %[rs], s64 offen sc1\n" \
    "buffer_load_dwordx4 v[196:199], %[voc], %[rs], s64 offen offset:2048 sc1\n" \
    "buffer_load_dwordx4 v[200:203], %[vod], %[rs], s64 offen sc1\n" \
    "buffer_load_dwordx4 v[204:207], %[vod], %[rs], s64 offen offset:2048 sc1\n" \
    "s_mov_b64 exec, -1\n" \
    "s_mov_b32 s67, 1\n" \
    "824:\n" \
    "v_max3_u32 v224, v208, v209, v210\n" \
    "v_max3_u32 v224, v224, v211, v212\n" \
    "v_max3_u32 v224, v224, v213, v214\n" \
    "v_max3_u32 v224, v224, v215, v216\n" \
    "v_max3_u32 v224, v224, v217, v218\n" \
    "v_max3_u32 v224, v224, v219, v220\n" \
    "v_max3_u32 v224, v224, v221, v222\n" \
    "v_max_u32 v224, v224, v223\n" \
    "v_cmp_eq_u32 vcc, -1, v224\n" \
    "s_cbranch_vccz 825f\n" \
    "s_mov_b64 exec, vcc\n" \
    "buffer_load_dwordx4 v[208:211], %[voc], %[rs], s65 offen sc1\n" \
    "buffer_load_dwordx4 v[212:215], %[voc], %[rs], s65 offen offset:2048 sc1\n" \
    "buffer_load_dwordx4 v[216:219], %[vod], %[rs], s65 offen sc1\n" \
    "buffer_load_dwordx4 v[220:223], %[vod], %[rs], s65 offen offset:2048 sc1\n" \
    "s_mov_b64 exec, -1\n" \
    "s_mov_b32 s67, 1\n" \
    "825:\n" \
    "s_cmp_eq_u32 s67, 0\n" \
    "s_cbranch_scc1 83f\n" \
    "s_sub_u32 s66, s66, 1\n" \
    "s_cbranch_scc0 81b\n" \
    "83:\n" \
    "s_waitcnt vmcnt(0)\n" \
    "s_cmp_eq_u32 s68, 0\n" \
    "s_cbranch_scc1 300b\n" \
    "s_cmp_eq_u32 s68, 1\n" \
    "s_cbranch_scc1 301b\n" \
    "s_cmp_eq_u32 s68, 2\n" \
    "s_cbranch_scc1 302b\n" \
    "s_cmp_eq_u32 s68, 3\n" \
    "s_cbranch_scc1 303b\n" \
    "s_cmp_eq_u32 s68, 4\n" \
    "s_cbranch_scc1 304b\n" \
    "s_cmp_eq_u32 s68, 5\n" \
    "s_cbranch_scc1 305b\n" \
    "s_cmp_eq_u32 s68, 6\n" \
    "s_cbranch_scc1 306b\n" \
    "s_cmp_eq_u32 s68, 7\n" \
    "s_cbranch_scc1 307b\n" \
    "s_cmp_eq_u32 s68, 8\n" \
    "s_cbranch_scc1 308b\n" \
    "s_cmp_eq_u32 s68, 9\n" \
    "s_cbranch_scc1 309b\n" \
    "s_cmp_eq_u32 s68, 10\n" \
    "s_cbranch_scc1 310b\n" \
    "s_branch 311b\n" \
    "99:\n" \
    "s_nop 7\n"
#define ASM_G2_CLOB "v128", "v129", "v130", "v131", "v132", "v133", "v134", "v135", "v136", "v137", "v138", "v139", "v140", "v141", "v142", "v143", "v144", "v145", "v146", "v147", "v148", "v149", "v150", "v151", "v152", "v153", "v154", "v155", "v156", "v157", "v158", "v159", "v160", "v161", "v162", "v163", "v164", "v165", "v166", "v167", "v168", "v169", "v170", "v171", "v172", "v173", "v174", "v175", "v176", "v177", "v178", "v179", "v180", "v181", "v182", "v183", "v184", "v185", "v186", "v187", "v188", "v189", "v190", "v191", "v192", "v193", "v194", "v195", "v196", "v197", "v198", "v199", "v200", "v201", "v202", "v203", "v204", "v205", "v206", "v207", "v208", "v209", "v210", "v211", "v212", "v213", "v214", "v215", "v216", "v217", "v218", "v219", "v220", "v221", "v222", "v223", "v224", "v228", "v229", "v230", "v231", "s60", "s61", "s62", "s63", "s64", "s65", "s66", "s67", "s68", "s69", "vcc", "scc", "memory"
__device__ __forceinline__ int col_bin(int c) {
    const int k1 = c >> 5, k2 = c & 31;
    const int a = k2 < 16 ? k1 + 20 * k2 : 640 - k1 - 20 * k2;
    return c < 16 ? 20 * c : c < 32 ? 10 + 20 * (c - 16) : a;
}
__device__ __forceinline__ float kap_bin(int kap) { return kap == 0 ? 320.0f : (float)col_bin(kap >> 1); }
__device__ __forceinline__ float kap_phase(int kap) { return (kap > 1 && (kap & 1)) ? 0.25f : 0.0f; }

constexpr int col_bin_c(int c) {
    return c < 16 ? 20 * c : c < 32 ? 10 + 20 * (c - 16) : (c & 31) < 16 ? (c >> 5) + 20 * (c & 31) : 640 - (c >> 5) - 20 * (c & 31);
}
constexpr int bin_col_c(int k) {
    return k == 320 ? 0 : k % 20 == 0 ? k / 20 : k % 20 == 10 ? 16 + k / 20 : k % 20 < 10 ? 32 * (k % 20) + k / 20 : 32 * (20 - k % 20) + 31 - k / 20;
}
constexpr double cos_small(double x) {
    double t = 1.0, s = 1.0;
    for (int i = 1; i <= 9; ++i) { t *= -x * x / ((2 * i - 1) * (2 * i)); s += t; }
    return s;
}
constexpr double sin_small(double x) {
    double t = x, s = x;
    for (int i = 1; i <= 9; ++i) { t *= -x * x / ((2 * i) * (2 * i + 1)); s += t; }
    return s;
}
struct Cos640 { double v[640]; };
constexpr Cos640 make_cos640() {
    Cos640 c = {};
    for (int m = 0; m < 640; ++m) {
        const int a = m <= 320 ? m : 640 - m;
        const bool neg = a > 160;
        const int b = neg ? 320 - a : a;
        const double v = b <= 80 ? cos_small(b * (3.14159265358979323846 / 320.0)) : sin_small((160 - b) * (3.14159265358979323846 / 320.0));
        c.v[m] = neg ? -v : v;
    }
    return c;
}
constexpr uint16_t bf16_c(double d) {
    const unsigned u = __builtin_bit_cast(unsigned, (float)d);
    return (uint16_t)((u + 0x7FFFu + ((u >> 16) & 1u)) >> 16);
}
struct PrepTab {
    uint16_t b1[40][64][8];
    uint16_t f20[2][64][8];
    uint16_t cmap[40][8];
};
constexpr PrepTab make_prep_tab() {
    const Cos640 c = make_cos640();
    PrepTab t = {};
    for (int tau = 0; tau < 40; ++tau)
        for (int l = 0; l < 64; ++l) {
            const int kap = 16 * tau + (l & 15), g = l >> 4;
            const int bin = kap == 0 ? 320 : col_bin_c(kap >> 1), ph = (kap > 1 && (kap & 1)) ? 160 : 0;
            const double sc = kap < 2 ? 0.70710678118654752 : 1.0;
            for (int j = 0; j < 8; ++j) t.b1[tau][l][j] = bf16_c(sc * c.v[((16 * (j >> 2) + 4 * g + (j & 3)) * bin + ph) % 640]);
        }
    for (int u = 0; u < 2; ++u)
        for (int l = 0; l < 64; ++l) {
            const int s = 16 * u + (l & 15), g = l >> 4;
            const int k1 = s == 0 ? 0 : s == 1 ? 10 : s >> 1, ph = (s > 1 && (s & 1)) ? 160 : 0;
            for (int j = 0; j < 8; ++j) {
                const int d1 = j < 4 ? 4 * g + j : 12 + 4 * g + j;
                t.f20[u][l][j] = (s < 20 && d1 < 20) ? bf16_c(0.055901699437494742 * c.v[(32 * d1 * k1 + ph) % 640]) : (uint16_t)0;
            }
        }
    for (int cg = 0; cg < 40; ++cg)
        for (int e = 0; e < 8; ++e) t.cmap[cg][e] = (uint16_t)bin_col_c(1 + 8 * cg + e);
    return t;
}
static __device__ const __attribute__((aligned(16))) PrepTab g_prep_tab = make_prep_tab();

struct Cos2048 { float v[2048]; };
constexpr Cos2048 make_cos2048() {
    Cos2048 c = {};
    for (int b = 0; b <= 512; ++b) {
        const double v = b <= 256 ? cos_small(b * (3.14159265358979323846 / 1024.0)) : sin_small((512 - b) * (3.14159265358979323846 / 1024.0));
        c.v[b] = (float)v;
        c.v[(2048 - b) & 2047] = (float)v;
        c.v[1024 - b] = (float)-v;
        c.v[(1024 + b) & 2047] = (float)-v;
    }
    return c;
}
constexpr Cos2048 k_cos2048 = make_cos2048();
constexpr unsigned bf16_f(float f) {
    const unsigned u = __builtin_bit_cast(unsigned, f);
    return (u + 0x7FFFu + ((u >> 16) & 1u)) >> 16;
}
struct CTab { unsigned v[16][64][4]; };
constexpr CTab make_ctab(int w) {
    CTab t = {};
    const float *c = k_cos2048.v;
    for (int l = 0; l < 64; ++l) {
        const int g = l >> 4, i = l & 15;
        for (int k = 0; k < 2; ++k)
            for (int h = 0; h < 4; ++h) {
                const int m0 = 32 * (32 * k + 2 * h + 8 * g) * (16 * w + i), m1 = m0 + 32 * (16 * w + i);
                t.v[k][l][h] = bf16_f(c[m0 & 2047]) | (bf16_f(c[m1 & 2047]) << 16);
                t.v[2 + k][l][h] = bf16_f(c[(m0 + 1536) & 2047]) | (bf16_f(c[(m1 + 1536) & 2047]) << 16);
                const int d0 = 16 * (h >> 1) + 2 * (h & 1) + 4 * g, n0 = 64 * d0 * (16 * k + i), n1 = n0 + 64 * (16 * k + i);
                t.v[4 + k][l][h] = bf16_f(c[n0 & 2047]) | (bf16_f(c[n1 & 2047]) << 16);
                t.v[6 + k][l][h] = bf16_f(c[(n0 + 1536) & 2047]) | (bf16_f(c[(n1 + 1536) & 2047]) << 16);
            }
        for (int e = 0; e < 8; ++e) {
            const int tb = e >> 2, r = e & 3;
            const int x1 = (16 * tb + r + 4 * g) * (16 * w + i), x2 = (i + 16 * tb) * (16 * w + 4 * g + r);
            t.v[8 + tb][l][r] = __builtin_bit_cast(unsigned, c[x1 & 2047]);
            t.v[10 + tb][l][r] = __builtin_bit_cast(unsigned, c[(x1 + 512) & 2047]);
            t.v[12 + tb][l][r] = __builtin_bit_cast(unsigned, c[x2 & 2047] * (1.0f / 2048.0f));
            t.v[14 + tb][l][r] = __builtin_bit_cast(unsigned, c[(x2 + 1536) & 2047] * (1.0f / 2048.0f));
        }
    }
    return t;
}
constexpr CTab k_ctab0 = make_ctab(0), k_ctab1 = make_ctab(1), k_ctab2 = make_ctab(2), k_ctab3 = make_ctab(3);
struct CTab4 { CTab w[4]; };
static __device__ const __attribute__((aligned(16))) CTab4 g_ctab = {{k_ctab0, k_ctab1, k_ctab2, k_ctab3}};

#define TSA(n)
constexpr int A_T = 0;
constexpr int A_Y = 41216;
constexpr int A_RED = A_Y + 41600;
constexpr int A_BYTES = 84 * 1024;

__device__ __forceinline__ void mf_two(f32x4 &c0, f32x4 &c1, v4u a, v4u b0, v4u b1) {
    asm volatile("s_nop 1\n\t" MFMA_OP "%0, %2, %3, 0\n\t" MFMA_OP "%1, %2, %4, 0" : "=&v"(c0), "=&v"(c1) : "v"(a), "v"(b0), "v"(b1));
}
#define PREP_AUX AUX_SC1
__device__ __forceinline__ void st_ws(v4u v, __amdgpu_buffer_rsrc_t rs, int voff, int soff) {
    __builtin_amdgcn_raw_buffer_store_b128(v, rs, voff + soff, 0, PREP_AUX);
}

__device__ __forceinline__ int step_ftile(int step, int hh) {
    const int jp = step / 6, s = step - 6 * jp;
    return 12 * jp + (s < 4 ? 3 * s + hh : 3 * (2 * (s - 4) + hh) + 2);
}
__device__ __forceinline__ void mark_empty(__amdgpu_buffer_rsrc_t rs, unsigned idx, unsigned cnt) {
    const v4u e = {EMPTY, EMPTY, EMPTY, EMPTY};
    for (unsigned it = idx * 256 + threadIdx.x; it < (OFF_PZ_END - OFF_PZ) / 16; it += cnt * 256) st_ws(e, rs, it * 16, OFF_PZ);
}

template <class Hook>
__device__ __forceinline__ void w1_task(const Params &p, LAS unsigned char *L, __amdgpu_buffer_rsrc_t rs, int task, int w, int lane, int tsid, Hook &&hook) {
    const int g = lane >> 4, n = lane & 15;
    v4u ld[10];
#pragma unroll
    for (int i = 0; i < 10; ++i) {
        const int rp = 16 * w + 64 * i + (lane >> 2), d2 = (rp * 3277) >> 16, d1 = rp - 20 * d2;
        ld[i] = *(const GAS v4u *)((const GAS unsigned char *)p.w1 + (32 * d1 + d2) * 3072 + task * 64 + (lane & 3) * 16);
    }
    __builtin_amdgcn_sched_barrier(0);
    const GAS unsigned char *tb = (const GAS unsigned char *)&g_prep_tab;
    v4u f20[2], b1[5][2], b2[5][2];
#pragma unroll
    for (int u = 0; u < 2; ++u) f20[u] = *(const GAS v4u *)(tb + 40960 + u * 1024 + lane * 16);
#pragma unroll
    for (int b = 0; b < 5; ++b)
#pragma unroll
        for (int u = 0; u < 2; ++u) b1[b][u] = *(const GAS v4u *)(tb + (2 * (5 * w + b) + u) * 1024 + lane * 16);
    __builtin_amdgcn_sched_barrier(0);
    TSA(1);
#pragma unroll
    for (int i = 0; i < 10; ++i) {
        const int rp = 16 * w + 64 * i + (lane >> 2), d2 = (rp * 3277) >> 16, rho = rp + (d2 >> 3), ch = lane & 3;
        *(LAS v4u *)(L + A_T + rho * 64 + (((ch >> 1) ^ ((rho >> 2) & 1)) * 32) + (ch & 1) * 16) = ld[i];
    }
    __builtin_amdgcn_sched_barrier(0);
    hook();
#pragma unroll
    for (int b = 0; b < 5; ++b)
#pragma unroll
        for (int u = 0; u < 2; ++u) {
            const int ks = 5 * w + b;
            const unsigned sg = (((ks & 1) ^ (n & 1) ^ 1) ? 0x80008000u : 0u);
#pragma unroll
            for (int h = 0; h < 4; ++h) {
                if (b == 0 && u == 0 && w == 0 && n == 1) b1[b][u][h] = 0u;
                const unsigned o = (unsigned)__builtin_amdgcn_update_dpp(0, (int)b1[b][u][h], 0xB1, 0xF, 0xF, true);
                b2[b][u][h] = ks < 2 ? 0u : o ^ sg;
            }
            asm volatile("" : "+v"(b2[b][u]));
        }
    __builtin_amdgcn_sched_barrier(0);
    TSA(3);
    __syncthreads();
    TSA(4);
    const int q4 = (lane >> 2) & 3, pi = lane & 3;
    {
        const int rl = 161 * w + 4 * g + q4, rh = 161 * w + 16 + q4;
        const int al = A_T + rl * 64 + ((rl >> 2) & 1) * 32 + pi * 8, ah = A_T + rh * 64 + ((rh >> 2) & 1) * 32 + pi * 8;
        const int yw = A_Y + n * 2080 + 8 * w * 64 + g * 8;
        v4u a[8][2];
#pragma unroll
        for (int e = 0; e < 8; ++e)
#pragma unroll
            for (int t = 0; t < 2; ++t) {
                const int x = ((e + t) & 1) * 32;
                const v2u lo = lds_tr(L, (al ^ x) + e * 1280), hi = lds_tr(L, (ah ^ x) + e * 1280);
                a[e][t] = v4u{lo.x, lo.y, hi.x, hi.y};
            }
        v2u lo16[8][2], hi4[8][2];
        f32x4 y[8][2][2];
        auto mm = [&](int e) {
#pragma unroll
            for (int t = 0; t < 2; ++t) mf_two(y[e][t][0], y[e][t][1], a[e][t], f20[0], f20[1]);
        };
        auto pk = [&](int e) {
#pragma unroll
            for (int t = 0; t < 2; ++t) {
                const f32x4 y0 = y[e][t][0], y1 = y[e][t][1];
                lo16[e][t] = v2u{pk_bf(y0[0], y0[1]), pk_bf(y0[2], y0[3])};
                hi4[e][t] = v2u{pk_bf(y1[0], y1[1]), pk_bf(y1[2], y1[3])};
            }
        };
        mm(0); mm(1);
#pragma unroll
        for (int e = 0; e < 6; ++e) {
            mm(e + 2);
            MF_FENCE0(y[e][0][0], y[e][0][1], y[e][1][0], y[e][1][1]);
            pk(e);
        }
        MF_FENCE4(y[7][0][0], y[7][0][1], y[7][1][0], y[7][1][1]);
        MF_FENCE0(y[6][0][0], y[6][0][1], y[6][1][0], y[6][1][1]);
        pk(6); pk(7);
#pragma unroll
        for (int e = 0; e < 8; ++e)
#pragma unroll
            for (int t = 0; t < 2; ++t) *(LAS v2u *)(L + yw + e * 64 + ((t ^ ((e >> 2) & 1)) * 32)) = lo16[e][t];
        if (n < 4) {
#pragma unroll
            for (int e = 0; e < 8; ++e)
#pragma unroll
                for (int t = 0; t < 2; ++t) *(LAS v2u *)(L + yw + 16 * 2080 + e * 64 + ((t ^ ((e >> 2) & 1)) * 32)) = hi4[e][t];
        }
    }
    TSA(5);
    __syncthreads();
    TSA(6);
    const int yr0 = A_Y + (4 * g + q4) * 64 + (g & 1) * 32 + pi * 8;
    v4u yra[5][2], yia[5][2];
#pragma unroll
    for (int b = 0; b < 5; ++b) {
        const int ks = 5 * w + b, sr = ks < 2 ? ks : ks & ~1, si = ks | 1;
#pragma unroll
        for (int t = 0; t < 2; ++t) {
            const int ar = (yr0 ^ (t * 32)) + sr * 2080, ai = (yr0 ^ (t * 32)) + si * 2080;
            const v2u rl = lds_tr(L, ar), rh = lds_tr(L, ar + 1024), il = lds_tr(L, ai), ih = lds_tr(L, ai + 1024);
            yra[b][t] = v4u{rl.x, rl.y, rh.x, rh.y};
            yia[b][t] = v4u{il.x, il.y, ih.x, ih.y};
        }
    }
    f32x4 oo[2][2][2];
    auto mm2 = [&](int b) {
#pragma unroll
        for (int u = 0; u < 2; ++u)
#pragma unroll
            for (int t = 0; t < 2; ++t) mf_cplx2(oo[b & 1][u][t], b1[b][u], yra[b][t], b2[b][u], yia[b][t]);
    };
    mm2(0);
#pragma unroll
    for (int b = 0; b < 5; ++b) {
        const int ks = 5 * w + b;
        f32x4(&o)[2][2] = oo[b & 1];
        if (b + 1 < 5) {
            mm2(b + 1);
            MF_FENCE0(o[0][0], o[0][1], o[1][0], o[1][1]);
        } else MF_FENCE4(o[0][0], o[0][1], o[1][0], o[1][1]);
#pragma unroll
        for (int t = 0; t < 2; ++t) {
            const int ft = 2 * task + t, j = ft / 12, r3 = ft - 12 * j, ww = r3 / 3, r = r3 - 3 * ww;
            const v4u v = {pk_bf(o[0][t][0], o[0][t][1]), pk_bf(o[0][t][2], o[0][t][3]), pk_bf(o[1][t][0], o[1][t][1]), pk_bf(o[1][t][2], o[1][t][3])};
            st_ws(v, rs, lane * 16, OFF_W1F + ((j * 4 + ww) * 60 + ks * 3 + r) * 1024);
        }
    }
}

constexpr int X_YS = 1032;
template <int KIND, class Next, class Hook, class XHook>
__device__ __forceinline__ void row_task(const Params &p, LAS unsigned char *L, __amdgpu_buffer_rsrc_t rs, int task, int count, Next &&next, int w, int lane, int tsid,
                                         Hook &&hook, XHook &&xh) {
    const int g = lane >> 4, n = lane & 15;
    const int rq = lane & 3, cc = lane >> 2;
    v4u ld[4];
    unsigned ls[4];
    auto data_ld = [&](int t) {
        const GAS unsigned char *xb = (const GAS unsigned char *)(KIND == 0 ? p.x : KIND == 2 ? p.w2 : p.b2);
        unsigned ro[4];
#pragma unroll
        for (int e = 0; e < 4; ++e)
            ro[e] = KIND == 0 ? pos2tok(16 * t + 4 * rq + e) * 1280
                  : KIND == 2 ? (16 * step_ftile(t >> 1, rq & 1) + 8 * (t & 1) + 4 * (rq >> 1) + e) * 1280 : 0;
#pragma unroll
        for (int e = 0; e < 4; ++e) ld[e] = *(const GAS v4u *)(xb + (ro[e] + (16 * w + cc) * 16));
#pragma unroll
        for (int e = 0; e < 4; ++e) ls[e] = *(const GAS unsigned *)(xb + (ro[e] + (64 + 4 * w + (cc & 3)) * 16 + (cc >> 2) * 4));
    };
    data_ld(task);
    __builtin_amdgcn_sched_barrier(0);
    const GAS unsigned char *tb = (const GAS unsigned char *)&g_prep_tab;
    v4u f20[2], b1[5][2], b2[5][2];
#pragma unroll
    for (int u = 0; u < 2; ++u) f20[u] = *(const GAS v4u *)(tb + 40960 + u * 1024 + lane * 16);
#pragma unroll
    for (int b = 0; b < 5; ++b)
#pragma unroll
        for (int u = 0; u < 2; ++u) b1[b][u] = *(const GAS v4u *)(tb + (2 * (5 * w + b) + u) * 1024 + lane * 16);
    __builtin_amdgcn_sched_barrier(0);
    hook();
    __builtin_amdgcn_sched_barrier(0);
    TSA(1);
    auto stage = [&]() {
        const int a0 = A_T + (161 * (cc & 3) + 4 * w + (cc >> 2)) * 32 + rq * 8;
#pragma unroll
        for (int e = 0; e < 8; ++e) {
            const unsigned sel = (e & 1) ? 0x07060302u : 0x05040100u;
            *(LAS v2u *)(L + a0 + e * 640) = v2u{__builtin_amdgcn_perm(ld[1][e >> 1], ld[0][e >> 1], sel), __builtin_amdgcn_perm(ld[3][e >> 1], ld[2][e >> 1], sel)};
        }
        const int a1 = A_T + (161 * (cc & 3) + 16 + w) * 32 + rq * 8 + (cc >> 2) * 1280;
#pragma unroll
        for (int e = 0; e < 2; ++e) {
            const unsigned sel = e ? 0x07060302u : 0x05040100u;
            *(LAS v2u *)(L + a1 + e * 640) = v2u{__builtin_amdgcn_perm(ls[1], ls[0], sel), __builtin_amdgcn_perm(ls[3], ls[2], sel)};
        }
        __builtin_amdgcn_sched_barrier(0);
    };
    stage();
    const int vst = task * XH0_TS + (160 * w + 16 * (g & 1) + n) * XH0_RS + (g >> 1) * 16;
    if constexpr (KIND == 0) {
        const v4u e = {EMPTY, EMPTY, EMPTY, EMPTY};
#pragma unroll
        for (int b = 0; b < 5; ++b) st_ws(e, rs, vst, OFF_XH0 + b * 32 * XH0_RS);
        if (threadIdx.x < 16) __builtin_amdgcn_raw_buffer_store_b32(EMPTY, rs, (16 * task + threadIdx.x) * 4, OFF_RS0, PREP_AUX);
    }
    __builtin_amdgcn_sched_barrier(0);
#pragma unroll
    for (int b = 0; b < 5; ++b)
#pragma unroll
        for (int u = 0; u < 2; ++u) {
            const int ks = 5 * w + b;
            const unsigned sg = (((ks & 1) ^ (n & 1) ^ 1) ? 0x80008000u : 0u);
#pragma unroll
            for (int h = 0; h < 4; ++h) {
                const unsigned o = (unsigned)__builtin_amdgcn_update_dpp(0, (int)b1[b][u][h], 0xB1, 0xF, 0xF, true);
                b2[b][u][h] = ks < 2 ? 0u : o ^ sg;
            }
            asm volatile("" : "+v"(b2[b][u]));
        }
    __builtin_amdgcn_sched_barrier(0);
    float ssq[4] = {0.f, 0.f, 0.f, 0.f};
    const int q4 = (lane >> 2) & 3, pi = lane & 3;
#pragma nounroll
    for (int r = 0;; ++r) {
        int nxt = task;
        if constexpr (KIND == 2) {
            if (r + 1 < count) { nxt = next(r); data_ld(nxt); }
            __builtin_amdgcn_sched_barrier(0);
        }
        xh(std::integral_constant<int, 0>{}, r);
        if (r == 0) { TSA(3); } else if (r == 1) { TSA(3 + 10); }
        __syncthreads();
        if (r == 0) { TSA(4); } else if (r == 1) { TSA(4 + 10); }
        {
            const int al = A_T + (161 * w + 4 * g + q4) * 32 + pi * 8, ah = A_T + (161 * w + 16 + q4) * 32 + pi * 8;
            const int yw = A_Y + n * X_YS + 8 * w * 32 + g * 8;
            v4u a[8];
#pragma unroll
            for (int e = 0; e < 8; ++e) {
                const v2u lo = lds_tr(L, al + e * 640), hi = lds_tr(L, ah + e * 640);
                a[e] = v4u{lo.x, lo.y, hi.x, hi.y};
            }
            v2u lo16[8], hi4[8];
            f32x4 y[8][2];
            auto mm = [&](int e) { mf_two(y[e][0], y[e][1], a[e], f20[0], f20[1]); };
            auto pk = [&](int e) {
                const f32x4 y0 = y[e][0], y1 = y[e][1];
                lo16[e] = v2u{pk_bf(y0[0], y0[1]), pk_bf(y0[2], y0[3])};
                hi4[e] = v2u{pk_bf(y1[0], y1[1]), pk_bf(y1[2], y1[3])};
            };
            mm(0); mm(1); mm(2);
#pragma unroll
            for (int e = 0; e < 5; ++e) {
                mm(e + 3);
                asm volatile("" : "+v"(y[e][0]), "+v"(y[e][1]));
                pk(e);
            }
            MF_FENCE4(y[6][0], y[6][1], y[7][0], y[7][1]);
            asm volatile("" : "+v"(y[5][0]), "+v"(y[5][1]));
            pk(5); pk(6); pk(7);
            xh(std::integral_constant<int, 1>{}, r);
#pragma unroll
            for (int e = 0; e < 8; ++e) *(LAS v2u *)(L + yw + e * 32) = lo16[e];
            if (n < 4) {
#pragma unroll
                for (int e = 0; e < 8; ++e) *(LAS v2u *)(L + yw + 16 * X_YS + e * 32) = hi4[e];
            }
        }
        if (r == 0) { TSA(5); } else if (r == 1) { TSA(5 + 10); }
        xh(std::integral_constant<int, 2>{}, r);
        __syncthreads();
        xh(std::integral_constant<int, 3>{}, r);
        if (r == 0) { TSA(6); } else if (r == 1) { TSA(6 + 10); }
        const int yr0 = A_Y + (4 * g + q4) * 32 + pi * 8;
        v4u yra[5], yia[5];
#pragma unroll
        for (int b = 0; b < 5; ++b) {
            const int ks = 5 * w + b, sr = ks < 2 ? ks : ks & ~1, si = ks | 1;
            const int ar = yr0 + sr * X_YS, ai = yr0 + si * X_YS;
            const v2u rl = lds_tr(L, ar), rh = lds_tr(L, ar + 512), il = lds_tr(L, ai), ih = lds_tr(L, ai + 512);
            yra[b] = v4u{rl.x, rl.y, rh.x, rh.y};
            yia[b] = v4u{il.x, il.y, ih.x, ih.y};
        }
        f32x4 oo[5][2];
        auto mm2 = [&](int b) {
#pragma unroll
            for (int u = 0; u < 2; ++u) mf_cplx2(oo[b][u], yra[b], b1[b][u], yia[b], b2[b][u]);
        };
        mm2(0); mm2(1);
#pragma unroll
        for (int b = 0; b < 5; ++b) {
            f32x4(&o)[2] = oo[b];
            if (b + 2 < 5) mm2(b + 2);
            if (b == 3) asm volatile("s_nop 7" : "+v"(oo[3][0]), "+v"(oo[3][1]), "+v"(oo[4][0]), "+v"(oo[4][1]));
            else asm volatile("" : "+v"(o[0]), "+v"(o[1]));
            if constexpr (KIND == 3) {
#pragma unroll
                for (int u = 0; u < 2; ++u)
                    if (g == 0) __builtin_amdgcn_raw_buffer_store_b32(__builtin_bit_cast(unsigned, o[u][0]), rs, (160 * w + 32 * b + 16 * u + n) * 4, OFF_B2P, PREP_AUX);
            } else {
                const auto s0 = __builtin_amdgcn_permlane16_swap(pk_bf(o[0][0], o[0][1]), pk_bf(o[1][0], o[1][1]), false, false);
                const auto s1 = __builtin_amdgcn_permlane16_swap(pk_bf(o[0][2], o[0][3]), pk_bf(o[1][2], o[1][3]), false, false);
                const v4u val = {s0[0], s1[0], s0[1], s1[1]};
                if constexpr (KIND == 2) {
                    const int s = task >> 1, ww = s / 12, kf = s - 12 * ww;
                    const int f0 = (2 * b) / 5 * 240 + (2 * b) % 5, f1 = (2 * b + 1) / 5 * 240 + (2 * b + 1) % 5;
                    st_ws(val, rs, (32 * (task & 1) + 16 * (g >> 1) + n) * 16 + (g & 1) * (f1 - f0) * 1024, OFF_W2F + ((8 * w + ww) * 60 + kf * 5 + f0) * 1024);
                } else {
                    st_ws(not_empty(val), rs, vst, OFF_XH0 + b * 32 * XH0_RS);
                    const bool dc = b == 0 && w == 0 && n == 1;
#pragma unroll
                    for (int e = 0; e < 4; ++e) {
                        const float z0 = dc ? 0.f : o[0][e], z1 = o[1][e];
                        ssq[e] += z0 * z0;
                        ssq[e] += z1 * z1;
                    }
                }
            }
        }
        if (r == 0) { TSA(10); } else if (r == 1) { TSA(11); }
        xh(std::integral_constant<int, 4>{}, r);
        if (KIND != 2 || r + 1 >= count) break;
        __builtin_amdgcn_sched_barrier(0);
        stage();
        task = nxt;
    }
    if constexpr (KIND != 0) return;
#pragma unroll
    for (int e = 0; e < 4; ++e) {
        float v = ssq[e];
        v += __builtin_bit_cast(float, __builtin_amdgcn_update_dpp(0, __builtin_bit_cast(int, v), 0xB1, 0xF, 0xF, true));
        v += __builtin_bit_cast(float, __builtin_amdgcn_update_dpp(0, __builtin_bit_cast(int, v), 0x4E, 0xF, 0xF, true));
        v += __builtin_bit_cast(float, __builtin_amdgcn_update_dpp(0, __builtin_bit_cast(int, v), 0x141, 0xF, 0xF, true));
        v += __builtin_bit_cast(float, __builtin_amdgcn_update_dpp(0, __builtin_bit_cast(int, v), 0x140, 0xF, 0xF, true));
        ssq[e] = v;
    }
    LAS float *red = (LAS float *)(L + A_RED);
    if (n == 0) *(LAS f32x4 *)(red + 16 * w + 4 * g) = f32x4{ssq[0], ssq[1], ssq[2], ssq[3]};
    __syncthreads();
    if (threadIdx.x < 16) {
        const int t = threadIdx.x;
        const float s = (red[t] + red[16 + t]) + (red[32 + t] + red[48 + t]);
        __builtin_amdgcn_raw_buffer_store_b32(min(__builtin_bit_cast(unsigned, __builtin_amdgcn_rsqf(s * (1.0f / 640.0f) + 1e-5f)), EMPTY - 1u), rs,
                                              (16 * task + t) * 4, OFF_RS0, PREP_AUX);
    }
}

template <class Hook>
__device__ __forceinline__ void rb2_task(__amdgpu_buffer_rsrc_t rs, int ks, int w, int lane, Hook &&hook) {
    const int g = lane >> 4, n = lane & 15;
    float kb[8], ph[8];
#pragma unroll
    for (int jj = 0; jj < 8; ++jj) {
        const int kap = 32 * ks + 16 * (jj >> 2) + 4 * g + (jj & 3);
        kb[jj] = kap_bin(kap);
        ph[jj] = kap_phase(kap);
    }
    const float sc01 = (ks == 0 && g == 0) ? 0.039528471f : 0.055901699f;
#pragma unroll
    for (int jh = 0; jh < 2; ++jh)
#pragma unroll
        for (int m = 0; m < 5; ++m) {
            if (jh == 1 && m == 0) hook();
            const int j = 2 * w + jh;
            const float d = (float)(80 * j + 16 * m + n);
            float c[8];
#pragma unroll
            for (int jj = 0; jj < 8; ++jj)
                c[jj] = __builtin_amdgcn_cosf(__builtin_amdgcn_fractf(kb[jj] * d * (1.0f / 640.0f)) + ph[jj]) * (jj < 2 ? sc01 : 0.055901699f);
            const v4u v = {pk_bf(c[0], c[1]), pk_bf(c[2], c[3]), pk_bf(c[4], c[5]), pk_bf(c[6], c[7])};
            st_ws(v, rs, lane * 16, OFF_RB2 + ((j * 4 + ks / 5) * 25 + (ks % 5) * 5 + m) * 1024);
        }
}

typedef v4u v4u_a2 __attribute__((aligned(2)));
template <class Hook>
__device__ __forceinline__ void wt_task(const Params &p, LAS unsigned char *L, __amdgpu_buffer_rsrc_t rs, int task, int ww, int lane, Hook &&hook) {
    const int w = task & 3, g = (task >> 2) & 3, part = task >> 4;
    const GAS uint16_t *src = (const GAS uint16_t *)(part ? p.wi : p.wr);
    const int tid = 64 * ww + lane;
    v4u t[20];
#pragma unroll
    for (int i = 0; i < 20; ++i) {
        const int idx = 256 * i + tid, row = (idx * 52429) >> 21, ch = idx - 40 * row, e = row >> 4;
        t[i] = *(const GAS v4u_a2 *)(src + (16 * w + (row & 15) + 64 * (16 * (e >> 2) + 4 * g + (e & 3))) * 321 + 1 + 8 * ch);
    }
    const int cl = lane & 15;
    v4u cm[3];
#pragma unroll
    for (int rd = 0; rd < 3; ++rd) {
        const int cg = 4 * (ww + 4 * rd) + (lane >> 4);
        cm[rd] = *(const GAS v4u *)((const GAS unsigned char *)&g_prep_tab + 43008 + (cg < 40 ? cg : 0) * 16);
    }
    __builtin_amdgcn_sched_barrier(0);
#pragma unroll
    for (int i = 0; i < 20; ++i) {
        const int idx = 256 * i + tid, row = (idx * 52429) >> 21, ch = idx - 40 * row;
        *(LAS v4u *)(L + row * 656 + ch * 16) = t[i];
    }
    hook();
    __syncthreads();
#pragma unroll
    for (int rd = 0; rd < 3; ++rd) {
        if (rd < 2 || ww < 2) {
            const int cg = 4 * (ww + 4 * rd) + (lane >> 4);
            v4u v[8];
#pragma unroll
            for (int e = 0; e < 8; ++e) v[e] = lds_rd128(L, (16 * e + cl) * 656 + cg * 16);
#pragma unroll
            for (int cc = 0; cc < 8; ++cc) {
                const unsigned sel = (cc & 1) ? 0x07060302u : 0x05040100u;
                v4u o;
#pragma unroll
                for (int d = 0; d < 4; ++d) o[d] = __builtin_amdgcn_perm(v[2 * d + 1][cc >> 1], v[2 * d][cc >> 1], sel);
                const unsigned c = (cc & 1) ? cm[rd][cc >> 1] >> 16 : cm[rd][cc >> 1] & 0xFFFFu;
                st_ws(o, rs, c * 8192 + (16 * g + cl) * 16, OFF_WT + part * 4096 + w * 1024);
            }
        }
    }
}

__device__ __forceinline__ void w2t_load(const Params &p, v4u (&t)[10], int s, int w, int lane) {
    const GAS unsigned char *src = (const GAS unsigned char *)p.w2 + (16 * step_ftile(s, w >> 1) + 8 * (w & 1)) * 1280;
#pragma unroll
    for (int i = 0; i < 10; ++i) t[i] = *(const GAS v4u *)(src + (64 * i + lane) * 16);
}
__device__ __forceinline__ void w2t_rest(LAS unsigned char *L, __amdgpu_buffer_rsrc_t rs, const v4u (&t)[10], int s, int w, int lane) {
#pragma unroll
    for (int i = 0; i < 10; ++i) {
        const int idx = 64 * i + lane, row = (idx * 52429) >> 22, ch = idx - 80 * row;
        *(LAS v4u *)(L + (8 * w + row) * 1312 + ch * 16) = t[i];
    }
    __syncthreads();
    const int g = lane >> 4, q4 = (lane >> 2) & 3, pi = lane & 3;
    const int a0 = (4 * g + q4) * 1312 + pi * 8;
    const int ww = s / 12, ks = s - 12 * ww;
#pragma unroll
    for (int i = 0; i < 10; ++i) {
        const int tau = 10 * w + i, j = tau / 5, m = tau - 5 * j;
        const v2u lo = lds_tr(L, a0 + tau * 32), hi = lds_tr(L, a0 + 16 * 1312 + tau * 32);
        st_ws(v4u{lo.x, lo.y, hi.x, hi.y}, rs, lane * 16, OFF_W2T + ((j * 4 + ww) * 60 + ks * 5 + m) * 1024);
    }
}
template <class Hook>
__device__ __forceinline__ void w2t_task(const Params &p, LAS unsigned char *L, __amdgpu_buffer_rsrc_t rs, int task, int w, int lane, Hook &&hook) {
    v4u t[20];
    const GAS unsigned char *src = (const GAS unsigned char *)p.w2 + 16 * step_ftile(2 * task + (w >> 1), w & 1) * 1280;
#pragma unroll
    for (int i = 0; i < 20; ++i) t[i] = *(const GAS v4u *)(src + (64 * i + lane) * 16);
    __builtin_amdgcn_sched_barrier(0);
#pragma unroll
    for (int i = 0; i < 20; ++i) {
        const int idx = 64 * i + lane, row = (idx * 52429) >> 22, ch = idx - 80 * row;
        *(LAS v4u *)(L + (16 * w + row) * 1312 + ch * 16) = t[i];
    }
    hook();
    __syncthreads();
    const int g = lane >> 4, q4 = (lane >> 2) & 3, pi = lane & 3;
    const int a0 = (4 * g + q4) * 1312 + pi * 8;
#pragma unroll
    for (int fg = 0; fg < 2; ++fg)
#pragma unroll
        for (int i = 0; i < 10; ++i) {
            const int tau = 10 * w + i, j = tau / 5, m = tau - 5 * j;
            const int grp = 2 * task + fg, ww = grp / 12, ks = grp - 12 * ww;
            const v2u lo = lds_tr(L, a0 + fg * 32 * 1312 + tau * 32), hi = lds_tr(L, a0 + (fg * 32 + 16) * 1312 + tau * 32);
            st_ws(v4u{lo.x, lo.y, hi.x, hi.y}, rs, lane * 16, OFF_W2T + ((j * 4 + ww) * 60 + ks * 5 + m) * 1024);
        }
}

constexpr int N_PZ = 40;

__device__ __forceinline__ bool any_lane(bool b) { return __builtin_amdgcn_ballot_w64(b) != 0ull; }
#define MEM_FENCE() asm volatile("" ::: "memory")
__device__ __forceinline__ v4u rs_words(const void *p, unsigned bytes) {
    const unsigned long long a = (unsigned long long)p;
    return v4u{(unsigned)a, (unsigned)(a >> 32) & 0xFFFFu, bytes, (unsigned)RSRC_W3};
}
__device__ __forceinline__ int fresh(int v) {
    asm volatile("" : "+v"(v));
    return v;
}

struct CConst {
    v4u Cr[2], Ci[2], nCi[2];
    v4u Er[2], Ei[2], nEi[2];
    f32x2 t1r[4], t1i[4], t2r[4], t2i[4];
};
template <int PART> __device__ __forceinline__ void c_consts(CConst &K, int w, int lane_) {
    const int lane = fresh(lane_);
    const int g = lane >> 4, i = lane & 15;
    if constexpr (PART == 0) {
        const float b = (float)(16 * w + i) * (1.0f / 64.0f), a0 = (float)(8 * g) * b;
#pragma unroll
        for (int ks = 0; ks < 2; ++ks) {
            float c[8], s[8];
#pragma unroll
            for (int j = 0; j < 8; ++j) {
                const float r = __builtin_fmaf((float)(32 * ks + j), b, a0);
                c[j] = __builtin_amdgcn_cosf(r);
                s[j] = __builtin_amdgcn_sinf(r);
            }
#pragma unroll
            for (int h = 0; h < 4; ++h) {
                K.Cr[ks][h] = pk_bf(c[2 * h], c[2 * h + 1]);
                K.nCi[ks][h] = pk_bf(s[2 * h], s[2 * h + 1]);
                K.Ci[ks][h] = K.nCi[ks][h] ^ 0x80008000u;
            }
        }
        asm volatile("" : "+v"(K.Cr[0]), "+v"(K.Cr[1]), "+v"(K.nCi[0]), "+v"(K.nCi[1]), "+v"(K.Ci[0]), "+v"(K.Ci[1]));
    } else if constexpr (PART == 1) {
#pragma unroll
        for (int td = 0; td < 2; ++td) {
            const float b = (float)(16 * td + i) * (1.0f / 32.0f), a0 = (float)(4 * g) * b;
            float c[8], s[8];
#pragma unroll
            for (int j = 0; j < 8; ++j) {
                const float r = __builtin_fmaf((float)(16 * (j >> 2) + (j & 3)), b, a0);
                c[j] = __builtin_amdgcn_cosf(r);
                s[j] = __builtin_amdgcn_sinf(r);
            }
#pragma unroll
            for (int h = 0; h < 4; ++h) {
                K.Er[td][h] = pk_bf(c[2 * h], c[2 * h + 1]);
                K.nEi[td][h] = pk_bf(s[2 * h], s[2 * h + 1]);
                K.Ei[td][h] = K.nEi[td][h] ^ 0x80008000u;
            }
        }
        asm volatile("" : "+v"(K.Er[0]), "+v"(K.Er[1]), "+v"(K.nEi[0]), "+v"(K.nEi[1]), "+v"(K.Ei[0]), "+v"(K.Ei[1]));
    } else {
        const float b1 = (float)(16 * w + i) * (1.0f / 2048.0f), a1 = (float)(4 * g) * b1;
        const float b2 = (float)(16 * w + 4 * g) * (1.0f / 2048.0f), i2 = (float)i;
#pragma unroll
        for (int e = 0; e < 8; ++e) {
            const int tb = e >> 2, r = e & 3;
            const float x1 = __builtin_fmaf((float)(16 * tb + r), b1, a1);
            K.t1r[e >> 1][e & 1] = __builtin_amdgcn_cosf(x1);
            K.t1i[e >> 1][e & 1] = __builtin_amdgcn_sinf(-x1);
            const float x2 = (i2 + (float)(16 * tb)) * (b2 + (float)r * (1.0f / 2048.0f));
            K.t2r[e >> 1][e & 1] = __builtin_amdgcn_cosf(x2) * (1.0f / 2048.0f);
            K.t2i[e >> 1][e & 1] = __builtin_amdgcn_sinf(x2) * (1.0f / 2048.0f);
        }
        asm volatile("" : "+v"(K.t1r[0]), "+v"(K.t1r[1]), "+v"(K.t1r[2]), "+v"(K.t1r[3]), "+v"(K.t1i[0]), "+v"(K.t1i[1]), "+v"(K.t1i[2]), "+v"(K.t1i[3]),
                     "+v"(K.t2r[0]), "+v"(K.t2r[1]), "+v"(K.t2r[2]), "+v"(K.t2r[3]), "+v"(K.t2i[0]), "+v"(K.t2i[1]), "+v"(K.t2i[2]), "+v"(K.t2i[3]));
    }
}
struct CRaw { v4u r[16]; };
template <int IMM> __device__ __forceinline__ void ld_imm(v4u &o, const GAS unsigned char *sbase, unsigned voff) {
    asm volatile("global_load_dwordx4 %0, %1, %2 offset:%c3" : "=v"(o) : "v"(voff), "s"(sbase), "i"(IMM));
}
__device__ __forceinline__ void c_tab_ld(CRaw &t, int w, int lane) {
    const GAS unsigned char *base = (const GAS unsigned char *)&g_ctab + w * 16384;
    static_for<0, 16>([&](auto sc) { ld_imm<(sc.value & 3) * 1024>(t.r[sc.value], base + (sc.value >> 2) * 4096, lane * 16); });
}
__device__ __forceinline__ void c_tab_use(CConst &K, CRaw &t) {
    asm volatile("" : "+v"(t.r[0]), "+v"(t.r[1]), "+v"(t.r[2]), "+v"(t.r[3]), "+v"(t.r[4]), "+v"(t.r[5]), "+v"(t.r[6]), "+v"(t.r[7]));
    asm volatile("" : "+v"(t.r[8]), "+v"(t.r[9]), "+v"(t.r[10]), "+v"(t.r[11]), "+v"(t.r[12]), "+v"(t.r[13]), "+v"(t.r[14]), "+v"(t.r[15]));
#pragma unroll
    for (int k = 0; k < 2; ++k) {
        K.Cr[k] = t.r[k]; K.nCi[k] = t.r[2 + k]; K.Ci[k] = t.r[2 + k] ^ 0x80008000u;
        K.Er[k] = t.r[4 + k]; K.nEi[k] = t.r[6 + k]; K.Ei[k] = t.r[6 + k] ^ 0x80008000u;
    }
#pragma unroll
    for (int h = 0; h < 4; ++h) {
        auto f = [&](int s) { return f32x2{u2f(t.r[s + (h >> 1)][(h & 1) * 2]), u2f(t.r[s + (h >> 1)][(h & 1) * 2 + 1])}; };
        K.t1r[h] = f(8); K.t1i[h] = f(10); K.t2r[h] = f(12); K.t2i[h] = f(14);
    }
}
template <bool RING> __device__ __forceinline__ void c_consts_ring(CConst &K, int w, int lane, const GAS unsigned char *run, unsigned voffr) {
    if constexpr (RING) ring_fill<0, 10>(run, voffr);
    c_consts<0>(K, w, lane);
    if constexpr (RING) ring_fill<10, 20>(run, voffr);
    c_consts<1>(K, w, lane);
    if constexpr (RING) ring_fill<20, 30>(run, voffr);
    c_consts<2>(K, w, lane);
}

__device__ __forceinline__ unsigned pk_bf2(f32x2 v) { return __builtin_bit_cast(unsigned, __builtin_convertvector(v, bf16x2)); }
__device__ __forceinline__ f32x2 bf_pair(unsigned u) { return f32x2{bf_lo(u), bf_hi(u)}; }
__device__ __forceinline__ f32x2 half2of(const f32x4 &v, int h) { return h ? f32x2{v[2], v[3]} : f32x2{v[0], v[1]}; }
__device__ __forceinline__ void cmul_pack(const f32x4 (&re)[2], const f32x4 (&im)[2], const f32x2 (&tr)[4], const f32x2 (&ti)[4], v4u &ore, v4u &oim) {
#pragma unroll
    for (int h = 0; h < 4; ++h) {
        const f32x2 x = half2of(re[h >> 1], h & 1), y = half2of(im[h >> 1], h & 1);
        ore[h] = pk_bf2(x * tr[h] - y * ti[h]);
        oim[h] = pk_bf2(x * ti[h] + y * tr[h]);
    }
}
#define FLG_W (i * 32 + j * 4 + w) * 4
#define FLG_R1 i * 128 + ((lane >> 1) & 7) * 16 + (lane & 1) * 8
#define FLG_R2 ((lc & 63) >> 1) * 128 + (q / 20) * 16 + (lc & 1) * 8
#define JPERM 0x53176420u
#define RS2 10
#define CTS1 -1
#define CTS2 -1
struct CIn {
    v4u xr[2], xi[2], wr[2], wi[2];
    f32x2 rstd[4];
};
__device__ __forceinline__ int xh_lane_off(int l) { return (l >> 3) * XH_BS + (l & 7) * 16; }

template <int RING, int TSB, bool LATEW = false, class Pre>
__device__ __forceinline__ void phase_c(const CConst &K, CIn &in, LAS unsigned char *L0, __amdgpu_buffer_rsrc_t rs, int q, unsigned xout, int w,
                                        int lane_, const GAS unsigned char *run, unsigned voffr, Pre &&pre) {
    auto ring_slot = [&](auto sc) {
        constexpr int s = sc.value;
        if constexpr (RING == 3 && s == 6) ring_fill<0, 36>(run, voffr);
        if constexpr (RING == 3 && s >= 1 && s < 5) ring_fill<30 + 6 * s, 36 + 6 * s>(run, voffr);
        if constexpr (RING == 1 && s < 5) ring_fill<30 + 6 * s, 36 + 6 * s>(run, voffr);
        if constexpr (RING == 2 && s >= 1 && s < 5) ring_fill<RS2 * (s - 1), RS2 * s>(run, voffr);
        if constexpr (RING == 2 && s == 5) ring_fill<RS2 * 4, 60>(run, voffr);
    };
    LAS unsigned char *L = L0 + L_PART;
#define TSC(n) do { if constexpr (TSB >= 0) { LAS unsigned char *L = L0; TS(TSB + (n)); } } while (0)
    const int lane = fresh(lane_);
    const int g = lane >> 4, bl = lane & 15;
    const int wadr = bl * 128 + (((2 * w + (g >> 1)) ^ ((bl >> 1) & 7)) * 16) + (g & 1) * 8;
    const int radr0 = bl * 128 + ((g ^ ((bl >> 1) & 7)) * 16), radr1 = bl * 128 + (((4 + g) ^ ((bl >> 1) & 7)) * 16);
    const bool col0 = (q == 0);
#pragma unroll
    for (int c = 0; c < 2; ++c) {
        v4u zr, zi;
#pragma unroll
        for (int h = 0; h < 4; ++h) {
            zr[h] = pk_bf2(bf_pair(in.xr[c][h]) * in.rstd[h]);
            zi[h] = pk_bf2(bf_pair(in.xi[c][h]) * in.rstd[h]);
            if (c == 0 && col0) zi[h] = 0u;
        }
#pragma unroll
        for (int t = 0; t < 2; ++t) {
            *(LAS v2u *)(L + c * 8192 + t * 2048 + wadr) = v2u{zr[2 * t], zr[2 * t + 1]};
            *(LAS v2u *)(L + c * 8192 + 4096 + t * 2048 + wadr) = v2u{zi[2 * t], zi[2 * t + 1]};
        }
    }
    ring_slot(std::integral_constant<int, 0>{});
    pre(std::integral_constant<int, 0>{});
    __syncthreads();
    ring_slot(std::integral_constant<int, 6>{});
    TSC(0);
    v4u vr[2], vi[2];
    {
        v4u r0[2][2], r1[2][2], i0[2][2], i1[2][2];
#pragma unroll
        for (int c = 0; c < 2; ++c)
#pragma unroll
            for (int tb = 0; tb < 2; ++tb) {
                r0[c][tb] = lds_rd128(L, c * 8192 + tb * 2048 + radr0);
                r1[c][tb] = lds_rd128(L, c * 8192 + tb * 2048 + radr1);
                i0[c][tb] = lds_rd128(L, c * 8192 + 4096 + tb * 2048 + radr0);
                i1[c][tb] = lds_rd128(L, c * 8192 + 4096 + tb * 2048 + radr1);
            }
        f32x4 ur[2][2], ui[2][2];
#pragma unroll
        for (int c = 0; c < 2; ++c)
#pragma unroll
            for (int tb = 0; tb < 2; ++tb) {
                mf_cplx4(ur[c][tb], r0[c][tb], K.Cr[0], r1[c][tb], K.Cr[1], i0[c][tb], K.nCi[0], i1[c][tb], K.nCi[1]);
                mf_cplx4(ui[c][tb], r0[c][tb], K.Ci[0], r1[c][tb], K.Ci[1], i0[c][tb], K.Cr[0], i1[c][tb], K.Cr[1]);
            }
        ring_slot(std::integral_constant<int, 1>{});
        MF_FENCE0(ur[0][0], ur[0][1], ui[0][0], ui[0][1]);
        cmul_pack(ur[0], ui[0], K.t1r, K.t1i, vr[0], vi[0]);
        MF_FENCE4(ur[1][0], ur[1][1], ui[1][0], ui[1][1]);
        cmul_pack(ur[1], ui[1], K.t1r, K.t1i, vr[1], vi[1]);
    }
    TSC(1);
    v4u yr[2], yi[2];
    {
        f32x4 Xr[2][2], Xi[2][2];
#pragma unroll
        for (int c = 0; c < 2; ++c)
#pragma unroll
            for (int td = 0; td < 2; ++td) {
                mf_cplx2(Xr[c][td], K.Er[td], vr[c], K.nEi[td], vi[c]);
                mf_cplx2(Xi[c][td], K.Ei[td], vr[c], K.Er[td], vi[c]);
            }
        f32x2 fr[2][4], fi[2][4];
        if constexpr (LATEW)
            asm volatile("s_waitcnt vmcnt(%4)" : "+v"(in.wr[0]), "+v"(in.wi[0]), "+v"(in.wr[1]), "+v"(in.wi[1]) : "i"(RING == 3 ? 42 : 0));
#pragma unroll
        for (int c = 0; c < 2; ++c)
#pragma unroll
            for (int h = 0; h < 4; ++h) { fr[c][h] = bf_pair(in.wr[c][h]); fi[c][h] = bf_pair(in.wi[c][h]); }
        ring_slot(std::integral_constant<int, 2>{});
        MF_FENCE0(Xr[0][0], Xr[0][1], Xi[0][0], Xi[0][1]);
        cmul_pack(Xr[0], Xi[0], fr[0], fi[0], yr[0], yi[0]);
        MF_FENCE4(Xr[1][0], Xr[1][1], Xi[1][0], Xi[1][1]);
        cmul_pack(Xr[1], Xi[1], fr[1], fi[1], yr[1], yi[1]);
    }
    TSC(2);
    {
        f32x4 pr[2][2], pi[2][2];
#pragma unroll
        for (int c = 0; c < 2; ++c)
#pragma unroll
            for (int tb = 0; tb < 2; ++tb) {
                mf_cplx2(pr[c][tb], yr[c], K.Er[tb], yi[c], K.Ei[tb]);
                mf_cplx2(pi[c][tb], yr[c], K.nEi[tb], yi[c], K.Er[tb]);
            }
        v4u pre[2], pim[2];
        ring_slot(std::integral_constant<int, 3>{});
        MF_FENCE0(pr[0][0], pr[0][1], pi[0][0], pi[0][1]);
        cmul_pack(pr[0], pi[0], K.t2r, K.t2i, pre[0], pim[0]);
        MF_FENCE4(pr[1][0], pr[1][1], pi[1][0], pi[1][1]);
        cmul_pack(pr[1], pi[1], K.t2r, K.t2i, pre[1], pim[1]);
#pragma unroll
        for (int c = 0; c < 2; ++c)
#pragma unroll
            for (int t = 0; t < 2; ++t) {
                *(LAS v2u *)(L + 16384 + c * 8192 + t * 2048 + wadr) = v2u{pre[c][2 * t], pre[c][2 * t + 1]};
                *(LAS v2u *)(L + 16384 + c * 8192 + 4096 + t * 2048 + wadr) = v2u{pim[c][2 * t], pim[c][2 * t + 1]};
            }
    }
    ring_slot(std::integral_constant<int, 4>{});
    TSC(3);
    pre(std::integral_constant<int, 1>{});
    __syncthreads();
    pre(std::integral_constant<int, 2>{});
    TSC(4);
    {
        v4u r0[2][2], r1[2][2], i0[2][2], i1[2][2];
#pragma unroll
        for (int c = 0; c < 2; ++c)
#pragma unroll
            for (int tb = 0; tb < 2; ++tb) {
                const int base = 16384 + c * 8192 + tb * 2048;
                r0[c][tb] = lds_rd128(L, base + radr0);
                r1[c][tb] = lds_rd128(L, base + radr1);
                i0[c][tb] = lds_rd128(L, base + 4096 + radr0);
                i1[c][tb] = lds_rd128(L, base + 4096 + radr1);
            }
        f32x4 or_[2][2], oi[2][2];
#pragma unroll
        for (int c = 0; c < 2; ++c)
#pragma unroll
            for (int tb = 0; tb < 2; ++tb) {
                mf_cplx4(or_[c][tb], K.Cr[0], r0[c][tb], K.Cr[1], r1[c][tb], K.Ci[0], i0[c][tb], K.Ci[1], i1[c][tb]);
                mf_cplx4(oi[c][tb], K.Cr[0], i0[c][tb], K.Cr[1], i1[c][tb], K.nCi[0], r0[c][tb], K.nCi[1], r1[c][tb]);
            }
        const int vx = xh_lane_off(64 * w + lane);
        TSC(5);
#pragma unroll
        for (int c = 0; c < 2; ++c) {
            if (c == 0) MF_FENCE0(or_[0][0], or_[0][1], oi[0][0], oi[0][1]);
            else MF_FENCE4(or_[1][0], or_[1][1], oi[1][0], oi[1][1]);
            v4u orr, oii;
#pragma unroll
            for (int h = 0; h < 4; ++h) {
                orr[h] = pk_bf2(bf_pair(in.xr[c][h]) + half2of(or_[c][h >> 1], h & 1));
                oii[h] = pk_bf2(bf_pair(in.xi[c][h]) + half2of(oi[c][h >> 1], h & 1));
            }
            if (c == 0 && col0) oii = in.xi[0];
            st_sc1(not_empty(orr), rs, vx, xout + (4 * q + 2 * c) * XH_RS);
            st_sc1(not_empty(oii), rs, vx, xout + (4 * q + 2 * c + 1) * XH_RS);
        }
    }
    ring_slot(std::integral_constant<int, 5>{});
}

__device__ __forceinline__ void ld_asm(v4u &o, const GAS unsigned char *sbase, unsigned voff) {
    asm volatile("global_load_dwordx4 %0, %1, %2" : "=v"(o) : "v"(voff), "s"(sbase));
}

__device__ __forceinline__ void ld_asm1(v4u &o, const GAS unsigned char *sbase, unsigned voff) {
    asm volatile("global_load_dwordx4 %0, %1, %2 sc1" : "=v"(o) : "v"(voff), "s"(sbase));
}

__device__ __forceinline__ void flags_ld(v2u &fl, const GAS unsigned char *wsg, unsigned voff) {
    asm volatile("global_load_dwordx2 %0, %1, %2 sc1" : "=v"(fl) : "v"(voff), "s"(wsg));
}
template <int NY> __device__ __forceinline__ void flags_wait(v2u &fl, const GAS unsigned char *wsg, unsigned voff) {
    asm volatile("s_waitcnt vmcnt(%1)" : "+v"(fl) : "i"(NY));
    for (unsigned spin = 0; spin < SPIN_CAP && any_lane(max(fl.x, fl.y) == EMPTY); ++spin)
        asm volatile("global_load_dwordx2 %0, %1, %2 sc1\n\ts_waitcnt vmcnt(0)" : "=v"(fl) : "v"(voff), "s"(wsg) : "memory");
}

__device__ __forceinline__ f32x2 gelu2(f32x2 x) {
    const f32x2 t = x * (x * x * -0.10294324f + -2.3022082f);
    const f32x2 d = f32x2{__builtin_amdgcn_exp2f(t[0]), __builtin_amdgcn_exp2f(t[1])} + 1.0f;
    return x * f32x2{__builtin_amdgcn_rcpf(d[0]), __builtin_amdgcn_rcpf(d[1])};
}
#define PIN2(a, b) __builtin_amdgcn_sched_barrier(0)
__device__ __forceinline__ v2u gelu4(const f32x4 &c, float rstd, v2u bias) {
    f32x2 x0 = half2of(c, 0) * rstd + bf_pair(bias.x), x1 = half2of(c, 1) * rstd + bf_pair(bias.y);
    PIN2(x0, x1);
    f32x2 t0 = x0 * x0, t1 = x1 * x1;
    PIN2(t0, t1);
    t0 = t0 * -0.10294324f + -2.3022082f;
    t1 = t1 * -0.10294324f + -2.3022082f;
    PIN2(t0, t1);
    t0 = x0 * t0;
    t1 = x1 * t1;
    PIN2(t0, t1);
    t0 = f32x2{__builtin_amdgcn_exp2f(t0[0]), __builtin_amdgcn_exp2f(t0[1])};
    t1 = f32x2{__builtin_amdgcn_exp2f(t1[0]), __builtin_amdgcn_exp2f(t1[1])};
    PIN2(t0, t1);
    t0 += 1.0f;
    t1 += 1.0f;
    PIN2(t0, t1);
    t0 = f32x2{__builtin_amdgcn_rcpf(t0[0]), __builtin_amdgcn_rcpf(t0[1])};
    t1 = f32x2{__builtin_amdgcn_rcpf(t1[0]), __builtin_amdgcn_rcpf(t1[1])};
    PIN2(t0, t1);
    t0 = x0 * t0;
    t1 = x1 * t1;
    PIN2(t0, t1);
    v2u y = {pk_bf2(t0), pk_bf2(t1)};
    PIN2(y.x, y.y);
    return y;
}
#define REF0 7
constexpr int REF_IN = 3 * (20 - REF0);
#define RA_EARLY 1
#define RA_PER 3
#define RA_REF0 9
#define RA_DEN 1
#define RA_TRAV 9
constexpr int RA_Q0 = RA_EARLY ? 3 * (20 - RA_REF0) + RA_TRAV : REF_IN;
static_assert(RA_Q0 <= 60 && RA_TRAV <= 29, "slots");
#define FLAG_STEP 12

#define LIGHT_FEW 24
__device__ __forceinline__ void stage_tile(LAS unsigned char *L, __amdgpu_buffer_rsrc_t rs, v4u rsv, unsigned xin, int i, bool light, int few, int w, int lane) {
    if (light) {
        const int row = 32 * (lane >> 1) + 8 * w + 4 * (lane & 1) + 3;
        const int voff = lane < 40 ? row * XH_RS + i * XH_BS : 0;
        for (unsigned spin = 0; spin < SPIN_CAP; ++spin) {
            MEM_FENCE();
            const unsigned v = __builtin_amdgcn_raw_buffer_load_b32(rs, voff, xin, AUX_SC1);
            if (__builtin_popcountll(__builtin_amdgcn_ballot_w64(lane < 40 && v == EMPTY)) <= few) break;
            __builtin_amdgcn_s_sleep(4);
        }
    }
    const int row = 8 * w + (lane >> 3), ch = lane & 7;
    const unsigned voff = row * XH_RS + i * XH_BS + ch * 16;
    const unsigned la = (unsigned)(size_t)(L + L_TILE) + row * 128 + (((ch >> 1) ^ ((row >> 1) & 3)) * 32) + (ch & 1) * 16, lb = la + 65536;
    asm volatile(ASM_STAGE : : [voff] "v"(voff), [la] "v"(la), [lb] "v"(lb), [rs] "s"(rsv), [xin] "s"(xin) : ASM_STAGE_CLOB);
}

template <bool LAST, int LATE, class Hook>
__device__ __forceinline__ void phase_g1(const Params &p, LAS unsigned char *L, __amdgpu_buffer_rsrc_t rs, v4u rsv, int i, int j, int w, int lane_,
                                         const GAS unsigned char *cur, const GAS unsigned char *nxt, bool light, v4u (&ra)[25], Hook &&hook) {
    const int lane = fresh(lane_);
    const int g = lane >> 4, n = lane & 15;
    v2u b1u[3];
#pragma unroll
    for (int r = 0; r < 3; ++r) b1u[r] = *(const GAS v2u *)((const GAS unsigned char *)p.b1 + (192 * j + 48 * w + 16 * r + 4 * g) * 2);
    hook(std::false_type{});
    stage_tile(L, rs, rsv, LAST ? OFF_XH3 : OFF_XH1, i, light, LIGHT_FEW, w, lane);
    hook(std::true_type{});
    asm volatile("" : "+v"(b1u[0]), "+v"(b1u[1]), "+v"(b1u[2]));
    DRAIN();
    __syncthreads();
    TS(LAST ? 15 : 4);
    constexpr int RF0 = LAST && RA_EARLY ? RA_REF0 : REF0, RF_IN = 3 * (20 - RF0);
    static_assert(!LAST || !RA_EARLY || RA_PER * RF0 >= 25, "the loads of R are older than the refills; slots of the rest");
    int tra[4];
    {
        const int q4 = (lane >> 2) & 3, pi = lane & 3, row = 4 * g + q4;
#pragma unroll
        for (int u = 0; u < 4; ++u) tra[u] = L_TILE + row * 128 + (((u ^ w) ^ ((row >> 1) & 3)) * 32) + pi * 8;
    }
    f32x4 acc[3][4], gram;
    float ssv[4];
    v4u b[2][4];
    v2u lo;
#pragma unroll
    for (int u = 0; u < 4; ++u) {
        const v2u l0 = lds_tr(L, tra[u]), h0 = lds_tr(L, tra[u] + 2048);
        b[0][u] = v4u{l0.x, l0.y, h0.x, h0.y};
    }
    const unsigned voffr = lane * 16;
    const unsigned hoff = (LAST ? OFF_H1 : OFF_H0) + i * (48 * 4096) + j * (6 * 4096);
    const v4u em = {EMPTY, EMPTY, EMPTY, EMPTY};
    const unsigned vx2 = (8 * w + (lane >> 3)) * XH_RS + (lane & 7) * 16 + OFF_XH2 + 80 * j * XH_RS + i * XH_BS;
    auto mark = [&](auto mc) {
        constexpr int m = mc.value;
        if constexpr (m < 4) st_sc1(em, rs, voffr, hoff + w * 4096 + m * 1024);
        else if constexpr (m < 6) st_sc1(em, rs, voffr, hoff + (4 + (w >> 1)) * 4096 + ((w & 1) * 2 + m - 4) * 1024);
        else if constexpr (m < 8) st_sc1(em, rs, vx2, (m - 6) * 32 * XH_RS);
        else if constexpr (m == 8) st_sc1(em, rs, vx2, ((w & 2) ? 48 : 64) * XH_RS);
    };
    static_for<0, 20>([&](auto kc) {
        constexpr int ks = kc.value;
        if constexpr (LATE == 30 && ks == 10) asm volatile("s_waitcnt vmcnt(24)" ::: "memory");
        if constexpr (LATE == 30 && ks == 15) asm volatile("s_waitcnt vmcnt(15)" ::: "memory");
        if constexpr (LATE == 45 && ks == 15) asm volatile("s_waitcnt vmcnt(24)" ::: "memory");
        static_for<0, 13>([&](auto nc) {
            constexpr int n_ = nc.value, mi = ks == 19 ? (n_ + 12) % 13 : n_, u = mi / 3, r = mi % 3;
            if constexpr (mi < 12) {
                if constexpr (ks == 0) mf_ra0<ks * 3 + r>(acc[r][u], b[ks & 1][u]);
                else mf_ra<ks * 3 + r>(acc[r][u], b[ks & 1][u]);
            } else if constexpr (ks == 0) {
                v4u m = b[0][0];
                if (g == 0) m.x &= 0x0000FFFFu;
                asm volatile("s_nop 1\n\t" MFMA_OP "%0, %1, %1, 0" : "=&v"(gram) : "v"(m));
            } else {
                asm volatile(MFMA_OP "%0, %1, %1, %0" : "+v"(gram) : "v"(b[ks & 1][0]));
            }
            if constexpr (n_ < 8 && ks + 1 < 20) {
                constexpr int u2 = n_ >> 1;
                if constexpr ((n_ & 1) == 0) lo = lds_tr(L, tra[u2] + (ks + 1) * 4096);
                else {
                    const v2u hi = lds_tr(L, tra[u2] + (ks + 1) * 4096 + 2048);
                    b[(ks + 1) & 1][u2] = v4u{lo.x, lo.y, hi.x, hi.y};
                }
            }
            if constexpr (n_ >= 8 && n_ < 11 && ks >= RF0) ring_ld_q<(ks - RF0) * 3 + n_ - 8>(nxt, voffr);
            if constexpr (LAST && RA_EARLY && n_ >= 8 && n_ < 8 + RA_PER && RA_PER * ks + n_ - 8 < 25) {
                constexpr int t = RA_PER * ks + n_ - 8;
                const GAS unsigned char *rb = (const GAS unsigned char *)p.ws + OFF_RB2 + ((j * 4 + w) * 25 + (t & ~3)) * 1024;
                asm volatile("global_load_dwordx4 %0, %1, %2 offset:%c3 sc1" : "=v"(ra[t]) : "v"(voffr), "s"(rb), "i"((t & 3) * 1024));
            }
            if constexpr (LATE == 45) {
                static_assert(REF0 == 7, "the waits above count the refills");
                constexpr int lq = ks < 5 ? (n_ == 8 ? 3 * ks : n_ == 10 ? 3 * ks + 1 : n_ == 12 ? 3 * ks + 2 : -1) : -1;
                if constexpr (lq >= 0) ring_ld_q<45 + lq>(cur, voffr);
            }
            if constexpr (LATE == 30) {
                static_assert(REF0 == 7, "the waits above count the refills");
                constexpr int lq = ks < 5 ? (n_ == 8 ? 3 * ks : n_ == 10 ? 3 * ks + 1 : n_ == 12 ? 3 * ks + 2 : -1)
                                 : ks < 7 ? (n_ >= 8 ? 15 + 5 * (ks - 5) + n_ - 8 : -1)
                                 : ks < 9 ? (n_ >= 11 ? 25 + 2 * (ks - 7) + n_ - 11 : -1) : (ks == 9 && n_ == 11) ? 29 : -1;
                if constexpr (lq >= 0) ring_ld_q<30 + lq>(cur, voffr);
            }
            if constexpr (!LAST && (n_ == 9 || n_ == 11) && ks < 5) mark(std::integral_constant<int, 2 * ks + (n_ == 11)>{});
            if constexpr (!LAST && n_ == 12 && ks == FLAG_STEP) {
                asm volatile("s_waitcnt vmcnt(%0)" ::"i"(3 * (FLAG_STEP + 1 - REF0)) : "memory");
                if (lane == 0) __builtin_amdgcn_raw_buffer_store_b32(1u, rs, OFF_FLG + FLG_W, 0, AUX_SC1);
            }
            if constexpr (ks == 19 && n_ == 4) {
                asm volatile("s_nop 3" : "+v"(gram));
                const int rr = n & 3;
                const float d = rr == 0 ? gram[0] : rr == 1 ? gram[1] : rr == 2 ? gram[2] : gram[3];
                if (g == (n >> 2)) ((LAS float *)(L + L_SS))[16 * w + n] = d;
            }
            if constexpr (ks == 19 && n_ == 8) asm volatile("s_waitcnt lgkmcnt(0)\n\ts_barrier" ::: "memory");
            if constexpr (ks == 19 && n_ == 9) {
#pragma unroll
                for (int k = 0; k < 4; ++k) ssv[k] = ((LAS float *)(L + L_SS))[16 * (k ^ w) + n];
            }
            __builtin_amdgcn_sched_barrier(0);
        });
    });
    asm volatile("s_nop 7" : "+v"(acc[0][0]), "+v"(acc[0][1]), "+v"(acc[0][2]), "+v"(acc[0][3]), "+v"(acc[1][0]), "+v"(acc[1][1]),
                 "+v"(acc[1][2]), "+v"(acc[1][3]), "+v"(acc[2][0]), "+v"(acc[2][1]), "+v"(acc[2][2]), "+v"(acc[2][3]));
    if constexpr (LAST && RA_EARLY) {
        asm volatile("s_waitcnt vmcnt(%13)" : "+v"(ra[0]), "+v"(ra[1]), "+v"(ra[2]), "+v"(ra[3]), "+v"(ra[4]), "+v"(ra[5]), "+v"(ra[6]), "+v"(ra[7]),
                     "+v"(ra[8]), "+v"(ra[9]), "+v"(ra[10]), "+v"(ra[11]), "+v"(ra[12]) : "i"(RF_IN));
        asm volatile("" : "+v"(ra[13]), "+v"(ra[14]), "+v"(ra[15]), "+v"(ra[16]), "+v"(ra[17]), "+v"(ra[18]), "+v"(ra[19]), "+v"(ra[20]), "+v"(ra[21]),
                     "+v"(ra[22]), "+v"(ra[23]), "+v"(ra[24]));
    }
    TS(LAST ? 16 : 5);
    constexpr int NTRAV = LAST ? (RA_EARLY ? RA_TRAV : 25) : 60 - RF_IN, PRE = NTRAV > 29 ? NTRAV - 24 : 5, FSLOT = 3;
    constexpr int DEN = LAST ? RA_DEN : 1;
    auto trav = [&](auto tc) {
        constexpr int t = tc.value;
        if constexpr (t < NTRAV) {
            if constexpr (LAST && !RA_EARLY) ra[t] = ld_sc1(rs, voffr, OFF_RB2 + ((j * 4 + w) * 25 + t) * 1024);
            else ring_ld_q<RF_IN + t>(nxt, voffr);
        }
    };
    static_for<0, PRE>(trav);
    __builtin_amdgcn_sched_barrier(0);
    float rstd[4];
#pragma unroll
    for (int u = 0; u < 4; ++u) rstd[u] = __builtin_amdgcn_rsqf(ssv[u] * (1.0f / 640.0f) + 1e-5f);
    v2u hp[3][4], fl;
    const unsigned vfl = OFF_FLG + FLG_R1;
    static_for<0, 4>([&](auto uc) {
        constexpr int u = uc.value;
        static_for<0, 3>([&](auto rc) {
            constexpr int r = rc.value, s0 = 6 * u + 2 * r;
            hp[r][u] = gelu4(acc[r][u], rstd[u], b1u[r]);
            static_for<0, DEN>([&](auto dc) { trav(std::integral_constant<int, PRE + DEN * s0 + dc.value>{}); });
            if constexpr (!LAST && s0 + 1 == FSLOT) flags_ld(fl, (const GAS unsigned char *)p.ws, vfl);
            static_for<0, DEN>([&](auto dc) { trav(std::integral_constant<int, PRE + DEN * (s0 + 1) + dc.value>{}); });
            if constexpr (r == 1) st_sc1(not_empty(v4u{hp[0][u].x, hp[0][u].y, hp[1][u].x, hp[1][u].y}), rs, voffr, hoff + w * 4096 + (u ^ w) * 1024);
            __builtin_amdgcn_sched_barrier(0);
        });
    });
#pragma unroll
    for (int pl = 0; pl < 2; ++pl) {
        const v2u e0 = hp[2][2 * pl], e1 = hp[2][2 * pl + 1];
        const v4u v = (w & 1) ? v4u{e1.x, e1.y, e0.x, e0.y} : v4u{e0.x, e0.y, e1.x, e1.y};
        st_sc1(not_empty(v), rs, voffr, hoff + (4 + (w >> 1)) * 4096 + ((w & 1) * 2 + (pl ^ (w >> 1))) * 1024);
    }
    if constexpr (!LAST) flags_wait<NTRAV - PRE - FSLOT + 6>(fl, (const GAS unsigned char *)p.ws, vfl);
}

__device__ __forceinline__ void phase_r(LAS unsigned char *L, int w, int lane_, const GAS unsigned char *nxt, v4u (&a)[25], f32x4 (&acc)[5][4]) {
    const int lane = fresh(lane_);
    const int g = lane >> 4;
    int tra[4];
    {
        const int q4 = (lane >> 2) & 3, pi = lane & 3, row = 4 * g + q4;
#pragma unroll
        for (int u = 0; u < 4; ++u) tra[u] = L_TILE + (5 * w) * 4096 + row * 128 + (((u ^ (w & 2)) ^ ((row >> 1) & 3)) * 32) + pi * 8;
    }
    v4u b[2][4];
    auto rd = [&](int ks, v4u(&o)[4]) {
#pragma unroll
        for (int u = 0; u < 4; ++u) {
            const v2u lo = lds_tr(L, tra[u] + ks * 4096), hi = lds_tr(L, tra[u] + ks * 4096 + 2048);
            o[u] = v4u{lo.x, lo.y, hi.x, hi.y};
        }
    };
    rd(0, b[0]);
    asm volatile("" : "+v"(a[0]), "+v"(a[1]), "+v"(a[2]), "+v"(a[3]), "+v"(a[4]), "+v"(a[5]), "+v"(a[6]), "+v"(a[7]), "+v"(a[8]), "+v"(a[9]),
                 "+v"(a[10]), "+v"(a[11]), "+v"(a[12]));
    asm volatile("" : "+v"(a[13]), "+v"(a[14]), "+v"(a[15]), "+v"(a[16]), "+v"(a[17]), "+v"(a[18]), "+v"(a[19]), "+v"(a[20]), "+v"(a[21]), "+v"(a[22]),
                 "+v"(a[23]), "+v"(a[24]));
    const unsigned voffr = lane * 16;
    static_for<0, 5>([&](auto kc) {
        constexpr int ks = kc.value;
        if constexpr (ks + 1 < 5) rd(ks + 1, b[(ks + 1) & 1]);
        static_for<0, 5>([&](auto mc) {
            constexpr int m = mc.value;
#pragma unroll
            for (int u = 0; u < 4; ++u) {
                if (ks == 0) mf_vv0(acc[m][u], a[ks * 5 + m], b[ks & 1][u]);
                else mf_vv(acc[m][u], a[ks * 5 + m], b[ks & 1][u]);
            }
            constexpr int q = RA_Q0 + ks * 5 + m;
            if constexpr (q < 60) ring_ld_q<q>(nxt, voffr);
        });
        __builtin_amdgcn_sched_barrier(0);
    });
    static_for<RA_Q0 + 25, 60>([&](auto q) { ring_ld_q<q.value>(nxt, voffr); });
}

template <bool LAST>
__device__ __forceinline__ void phase_g2(const Params &p, LAS unsigned char *L, __amdgpu_buffer_rsrc_t rs, v4u rsv, int i, int j, int w, int lane_,
                                         f32x4 (&acc)[5][4]) {
    int lane = fresh(lane_);
    v2u b2u[5];
    float b2f[5];
    const unsigned hoff = (LAST ? OFF_H1 : OFF_H0) + i * (48 * 4096) + (12 * w) * 4096;
    {
        const int g = lane >> 4, n = lane & 15;
#pragma unroll
        for (int m = 0; m < 5; ++m) {
            if constexpr (LAST) b2u[m] = *(const GAS v2u *)((const GAS unsigned char *)p.b2 + (80 * j + 16 * m + 4 * g) * 2);
            else b2f[m] = u2f(__builtin_amdgcn_raw_buffer_load_b32(rs, (80 * j + 16 * m + n) * 4, OFF_B2P, AUX_SC1));
        }
    }
    {
        const unsigned voa = lane * 16 + (w & 2) * 1024, vob = lane * 16 + ((w & 2) ^ 2) * 1024;
        const unsigned voc = lane * 16 + (w >> 1) * 1024, vod = lane * 16 + ((w >> 1) ^ 1) * 1024;
#define G2_ACC(c) [c00] c(acc[0][0]), [c01] c(acc[0][1]), [c02] c(acc[0][2]), [c03] c(acc[0][3]), [c10] c(acc[1][0]), [c11] c(acc[1][1]),            \
                  [c12] c(acc[1][2]), [c13] c(acc[1][3]), [c20] c(acc[2][0]), [c21] c(acc[2][1]), [c22] c(acc[2][2]), [c23] c(acc[2][3]),       \
                  [c30] c(acc[3][0]), [c31] c(acc[3][1]), [c32] c(acc[3][2]), [c33] c(acc[3][3]), [c40] c(acc[4][0]), [c41] c(acc[4][1]),       \
                  [c42] c(acc[4][2]), [c43] c(acc[4][3])
        if constexpr (LAST)
            asm volatile(ASM_G2_LAST : G2_ACC("+v")
                         : [voa] "v"(voa), [vob] "v"(vob), [voc] "v"(voc), [vod] "v"(vod), [rs] "s"(rsv), [hoff] "s"(hoff) : ASM_G2_CLOB);
        else {
            const unsigned vh1 = w * 1024 + lane * 16, h1off = OFF_H1 + i * (48 * 4096) + j * (6 * 4096);
            const unsigned vx3 = (8 * w + (lane >> 3)) * XH_RS + (lane & 7) * 16, vx4 = vx3 + ((w & 2) ? 48 : 64) * XH_RS;
            const unsigned x3off = OFF_XH3 + 80 * j * XH_RS + i * XH_BS;
            asm volatile(ASM_G2_MID : G2_ACC("=&v")
                         : [voa] "v"(voa), [vob] "v"(vob), [voc] "v"(voc), [vod] "v"(vod), [rs] "s"(rsv), [hoff] "s"(hoff), [vh1] "v"(vh1),
                           [h1off] "s"(h1off), [vx3] "v"(vx3), [vx4] "v"(vx4), [x3off] "s"(x3off)
                         : ASM_G2_CLOB);
        }
    }
    TS(LAST ? 20 : 9);
    lane = fresh(lane);
    const int g = lane >> 4, n = lane & 15;
    f32x4 sum[5];
    auto send = [&](auto uc) {
        constexpr int u = uc.value;
        const int t = u ^ (w & 2), slot = ((w - t) & 3) - 1;
#pragma unroll
        for (int m = 0; m < 5; ++m) *(LAS f32x4 *)(L + L_PART + ((t * 3 + slot) * 5 + m) * 1024 + lane * 16) = acc[m][u];
    };
    if (w & 1) {
        send(std::integral_constant<int, 0>{});
#pragma unroll
        for (int m = 0; m < 5; ++m) sum[m] = acc[m][1];
    } else {
        send(std::integral_constant<int, 1>{});
#pragma unroll
        for (int m = 0; m < 5; ++m) sum[m] = acc[m][0];
    }
    send(std::integral_constant<int, 2>{});
    send(std::integral_constant<int, 3>{});
    if constexpr (!LAST) DRAIN();
    __syncthreads();
    TS(LAST ? 22 : 10);
    f32x4 part[3][5];
#pragma unroll
    for (int s = 0; s < 3; ++s)
#pragma unroll
        for (int m = 0; m < 5; ++m) part[s][m] = *(LAS f32x4 *)(L + L_PART + ((w * 3 + s) * 5 + m) * 1024 + lane * 16);
    if constexpr (!LAST) {
        int tadr[5];
        v2u res[5];
#pragma unroll
        for (int m = 0; m < 5; ++m) {
            const int row = 80 * j + 16 * m + n;
            tadr[m] = L_TILE + row * 128 + ((w ^ ((row >> 1) & 3)) * 32) + g * 8;
            res[m] = *(LAS v2u *)(L + tadr[m]);
        }
        f32x2 ss0 = {0.f, 0.f}, ss1 = {0.f, 0.f};
#pragma unroll
        for (int m = 0; m < 5; ++m) {
            const f32x4 s = (sum[m] + part[0][m]) + (part[1][m] + part[2][m]);
            const v2u o = {pk_bf2(half2of(s, 0) + bf_pair(res[m].x) + b2f[m]), pk_bf2(half2of(s, 1) + bf_pair(res[m].y) + b2f[m])};
            *(LAS v2u *)(L + tadr[m]) = o;
            f32x2 z0 = bf_pair(o.x), z1 = bf_pair(o.y);
            if (m == 0 && j == 0 && n == 1) { z0 = f32x2{0.f, 0.f}; z1 = z0; }
            ss0 += z0 * z0;
            ss1 += z1 * z1;
        }
        float sq[4] = {ss0[0], ss0[1], ss1[0], ss1[1]};
#pragma unroll
        for (int e = 0; e < 4; ++e) {
            float v = sq[e];
            v += __builtin_bit_cast(float, __builtin_amdgcn_update_dpp(0, __builtin_bit_cast(int, v), 0x128, 0xF, 0xF, true));
            v += __builtin_bit_cast(float, __builtin_amdgcn_update_dpp(0, __builtin_bit_cast(int, v), 0x124, 0xF, 0xF, true));
            v += __builtin_bit_cast(float, __builtin_amdgcn_update_dpp(0, __builtin_bit_cast(int, v), 0x122, 0xF, 0xF, true));
            v += __builtin_bit_cast(float, __builtin_amdgcn_update_dpp(0, __builtin_bit_cast(int, v), 0x121, 0xF, 0xF, true));
            sq[e] = v;
        }
        if (n == 0) *(LAS v2u *)(L + L_SSB + (16 * w + 4 * g) * 2) = v2u{pk_bf(sq[0], sq[1]), pk_bf(sq[2], sq[3])};
        __syncthreads();
        v4u o[3];
#pragma unroll
        for (int rd_ = 0; rd_ < 3; ++rd_) {
            int it = (rd_ * 4 + w) * 64 + lane;
            if (it >= 640) it = lane;
            const int row = 80 * j + (it >> 3), ch = it & 7;
            o[rd_] = lds_rd128(L, L_TILE + row * 128 + (((ch >> 1) ^ ((row >> 1) & 3)) * 32) + (ch & 1) * 16);
        }
        const v4u sb = lds_rd128(L, L_SSB + (lane & 7) * 16);
#pragma unroll
        for (int rd_ = 0; rd_ < 3; ++rd_) {
            const int it = (rd_ * 4 + w) * 64 + lane;
            if (rd_ < 2 || w < 2) st_sc1(not_empty(o[rd_]), rs, (80 * j + (it >> 3)) * XH_RS + i * XH_BS + (it & 7) * 16, OFF_XH2);
        }
        if (w == 3 && lane < 8) st_sc1(not_empty(sb), rs, (j * 256 + 8 * i + lane) * 16, OFF_SSP);
    } else {
#pragma unroll
        for (int m = 0; m < 5; ++m) {
            const f32x4 s = (sum[m] + part[0][m]) + (part[1][m] + part[2][m]);
            *(LAS v2u *)(L + L_TILE + (16 * w + n) * 176 + (16 * m + 4 * g) * 2) =
                v2u{pk_bf2(half2of(s, 0) + bf_pair(b2u[m].x)), pk_bf2(half2of(s, 1) + bf_pair(b2u[m].y))};
        }
        __syncthreads();
        v4u o[3];
#pragma unroll
        for (int rd_ = 0; rd_ < 3; ++rd_) {
            int it = (rd_ * 4 + w) * 64 + lane;
            if (it >= 640) it = lane;
            const int tok = it / 10, ch = it - tok * 10;
            o[rd_] = lds_rd128(L, L_TILE + tok * 176 + ch * 16);
        }
#pragma unroll
        for (int rd_ = 0; rd_ < 3; ++rd_) {
            const int it = (rd_ * 4 + w) * 64 + lane;
            if (rd_ < 2 || w < 2) {
                const int tok = it / 10, ch = it - tok * 10;
                *(GAS v4u *)((GAS unsigned char *)p.out + pos2tok(64 * i + tok) * 1280 + (80 * j + 8 * ch) * 2) = o[rd_];
            }
        }
    }
}

template <int PB, int PE, bool FUSED> __global__ void __launch_bounds__(256, 1) k_main(Params p) {
    extern __shared__ __attribute__((aligned(16))) unsigned char smem[];
    LAS unsigned char *L = (LAS unsigned char *)smem;
    asm volatile("" ::: "a239");
    auto code_fetch = [&](unsigned idx) {
        unsigned long long pc;
        unsigned back, len;
        asm volatile("s_getpc_b64 %0\n.Lk_here_%=:\n\ts_mov_b32 %1, .Lk_here_%=-.Lk_begin_%c3\n\ts_mov_b32 %2, .Lk_end_%c3-.Lk_begin_%c3"
                     : "=s"(pc), "=s"(back), "=s"(len) : "i"(PB * 100 + PE * 10 + FUSED));
        const unsigned off = min(idx * 4096u + (threadIdx.x & 63u) * 64u, len - 4u);
        asm volatile("global_load_dword a240, %0, %1" : : "v"(off), "s"(pc - back) : "a240");
    };
    if constexpr (FUSED) asm volatile(".Lk_begin_%c0:" ::"i"(PB * 100 + PE * 10 + FUSED));
    if constexpr (FUSED) {
        const unsigned sl = blockIdx.x >> 3, z = 3u * (sl >> 3) + (sl & 7u) - 5u;
        if ((sl & 7u) >= 5u && z >= 6u && blockIdx.x != 255u) {
            const unsigned idx = (z - 6u) * 4u + (threadIdx.x >> 6);
            code_fetch(idx);
            if (idx < 16u)
                asm volatile("global_load_dword a240, %0, %1" : : "v"(idx * 4096u + (threadIdx.x & 63u) * 64u), "s"((const GAS unsigned char *)&g_ctab) : "a240");
        }
    }
    const __amdgpu_buffer_rsrc_t rs = __builtin_amdgcn_make_buffer_rsrc((void *)p.ws, 0, (int)WS_USED, RSRC_W3);
    const v4u rsv = rs_words(p.ws, WS_USED);
    const int lane = threadIdx.x & 63, w = __builtin_amdgcn_readfirstlane(threadIdx.x >> 6);
    const int bid = blockIdx.x, slot = bid >> 3;
    const int i = 4 * (bid & 7) + (slot >> 3), j = (JPERM >> (4 * (slot & 7))) & 7;
    const GAS unsigned char *wsg = (const GAS unsigned char *)p.ws;
    const GAS unsigned char *run1 = wsg + OFF_W1F + (j * 4 + w) * 61440;
    const GAS unsigned char *run2 = wsg + OFF_W2F + (j * 4 + w) * 61440;
    const GAS unsigned char *run3 = wsg + OFF_W2T + (j * 4 + w) * 61440;
    const unsigned voffr = lane * 16;
    const bool isC = (slot & 7) < 5;
    const int q = 5 * i + (slot & 7);
    TS(0);

#define XLINE
#define LIGHT1 (PB == 0 && !isC)
#define LIGHT2 (PB < 3 && !isC)
#define G2_DELAY __builtin_amdgcn_s_sleep(12)
#define T2_DELAY
#define X_DELAY
#define RING0 45
#define PF1 3
#define LATE1 60
#define LATE2 60
#define PD_SLEEP __builtin_amdgcn_s_sleep(8)
#define LATE_DELAY
#define PZ_AT 4
#define W2_DELAY __builtin_amdgcn_s_sleep(20)
#define W1_DELAY __builtin_amdgcn_s_sleep(20)
#define PD_FEW 3
#define XGATE 64
#define XSLEEP __builtin_amdgcn_s_sleep(4)
#define NC_SLEEP
    v4u pd = {0u, 0u, 0u, 0u};
    const unsigned vpd = OFF_PD + (bid & 7) * (64 * PD_ST) + lane * PD_ST;
    auto pd_poll = [&](unsigned va) { asm volatile("global_load_dwordx4 %0, %1, %2 sc1\n\ts_waitcnt vmcnt(0)" : "=v"(pd) : "v"(va), "s"(wsg) : "memory"); };
    auto pd_ask = [&](unsigned va) { asm volatile("global_load_dwordx4 %0, %1, %2 sc1" : "=v"(pd) : "v"(va), "s"(wsg)); };
    auto pd_wait = [&](int lo, int hi, unsigned va) {
        for (unsigned spin = 0; spin < SPIN_CAP && any_lane(lane >= lo && lane < hi && max(max(pd.x, pd.y), max(pd.z, pd.w)) == EMPTY); ++spin) {
            if (__builtin_popcountll(__builtin_amdgcn_ballot_w64(max(max(pd.x, pd.y), max(pd.z, pd.w)) == EMPTY && lane >= lo && lane < hi)) > PD_FEW) PD_SLEEP;
            pd_poll(va);
        }
    };
    if constexpr (FUSED) {
        const int jj = slot & 7, ihi = slot >> 3, xc = bid & 7;
        const int z = jj < 5 ? 5 * ihi + jj : 3 * ihi + jj - 5, v = 8 * z + xc;
        const int tsid = jj < 5 ? v : 160 + v;
        TSA(0);
        unsigned a0;
        asm volatile("s_load_dword %0, %1, 0x0" : "=s"(a0) : "s"(wsg + OFF_ACK + flag_off(bid, RQ_ST)));
        auto token = [&]() {
            unsigned r;
            asm volatile("s_waitcnt lgkmcnt(0)\n\ts_add_u32 %0, %1, 1" : "=s"(r) : "s"(a0) : "scc");
            return r;
        };
        auto request = [&]() {
            const unsigned r = token();
            if (w == 0 && lane == 0) __builtin_amdgcn_raw_buffer_store_b32(r, rs, flag_off(bid, RQ_ST), OFF_REQ, AUX_SC1);
        };
        auto answer = [&]() {
            const unsigned r = token();
            for (unsigned spin = 0; spin < SPIN_CAP; ++spin) {
                MEM_FENCE();
                if (__builtin_amdgcn_raw_buffer_load_b32(rs, flag_off(bid, RQ_ST), OFF_ACK, AUX_SC1) == r) break;
            }
        };
        auto raise = [&]() { if (lane < 8) __builtin_amdgcn_raw_buffer_store_b32(1u, rs, lane * (64 * PD_ST) + flag_off(tsid, PD_ST), OFF_PD, AUX_SC1); };
        auto ack_ask = [&](unsigned &o) { asm volatile("global_load_dword %0, %1, %2 sc1" : "=v"(o) : "v"(OFF_ACK + flag_off(bid, RQ_ST)), "s"(wsg)); };
        if (tsid >= 208 && tsid != 255) {
            const int l = v - 48;
            W2_DELAY;
            if (__builtin_expect(l == 43, 0)) {
                row_task<3>(p, L, rs, 0, 1, [](int) { return 0; }, w, lane, tsid, request, [](auto, int) {});
                __syncthreads();
            }
            unsigned acks = 0u;
            const unsigned vack = OFF_ACK + flag_off(bid, RQ_ST);
            bool raised = false;
            const int rp = l == 43 ? 0 : 1;
            int first = l == 43 ? 47 + l : l;
            asm volatile("" : "+s"(first));
            row_task<2>(p, L, rs, first, l == 43 ? 1 : l > 43 ? 3 : 2, [&](int r) { return r == 0 ? 47 + l : l == 44 ? 43 : l == 45 ? 95 : 94; }, w, fresh(lane), tsid,
                        [&]() { if (l != 43) request(); }, [&](auto kc, int r) {
                if (kc.value == PZ_AT && r == 0 && l < N_PZ) mark_empty(rs, l, N_PZ);
                if (r == rp) {
                    if (kc.value == 1) asm volatile("global_load_dword a241, %0, %1 sc1" : : "v"(vack), "s"(wsg) : "a241");
                    if (kc.value == 2) {
                        unsigned tmp;
                        asm volatile("s_waitcnt vmcnt(0)\n\tv_accvgpr_read_b32 %1, a241\n\ts_nop 0\n\tv_readfirstlane_b32 %0, %1" : "=s"(acks), "=&v"(tmp) : : "memory");
                    }
                    if (kc.value == 3 && w == 0 && acks == token()) { raise(); raised = true; TSA(2); }
                }
            });
            TSA(7);
            if (w == 0 && !raised) answer();
            DRAIN();
            __syncthreads();
            if (w == 0) {
                if (!raised) raise();
                if (lane < 8) __builtin_amdgcn_raw_buffer_store_b32(1u, rs, lane * (32 * P2_ST) + flag_off(l, P2_ST), OFF_PD2, AUX_SC1);
            }
            TSA(8);
        } else if (tsid >= 160 && tsid < 208) {
            W1_DELAY;
            w1_task(p, L, rs, 2 * ((v >> 4) * 8 + (v & 7)) + ((v >> 3) & 1), w, lane, tsid, request);
            TSA(7);
            if (w == 0) answer();
            DRAIN();
            __syncthreads();
            if (w == 0) raise();
            TSA(8);
        } else if (tsid == 255) {
            if (w == 0) {
                const v4u e = {EMPTY, EMPTY, EMPTY, lane == 63 ? 1u : EMPTY};
#pragma unroll
                for (int r = 0; r < 8; ++r) st_sc1(e, rs, lane * PD_ST, OFF_PD + r * (64 * PD_ST));
                const v4u e2 = {EMPTY, EMPTY, EMPTY, (lane & 31) == 11 ? 1u : EMPTY};
#pragma unroll
                for (int r = 0; r < 4; ++r) st_sc1(e2, rs, lane * P2_ST, OFF_PD2 + r * (64 * P2_ST));
                DRAIN();
            }
            __syncthreads();
            code_fetch(20u + w);
#define LD_COND
#define LD_COPY 7
#define LD_WAVES 2
#define LD_STAG 20
#define LD_SLEEP
            for (int k = 0; k < w; ++k) __builtin_amdgcn_s_sleep(LD_STAG);
            if (w < LD_WAVES) {
                v4u last = {0u, 0u, 0u, 0u};
                bool first = true;
                for (unsigned spin = 0; spin < SPIN_CAP; ++spin) {
                    MEM_FENCE();
                    const v4u rq = ld_sc1(rs, lane * RQ_ST, OFF_REQ), dn = ld_sc1(rs, lane * PD_ST, OFF_PD + LD_COPY * (64 * PD_ST));
                    if (first || rq.x != last.x || rq.y != last.y || rq.z != last.z || rq.w != last.w) st_sc1(rq, rs, lane * RQ_ST, OFF_ACK);
                    last = rq; first = false;
                    if (!any_lane(max(max(dn.x, dn.y), max(dn.z, dn.w)) == EMPTY)) break;
                    LD_SLEEP;
                }
            }
            DRAIN();
            __syncthreads();
        } else if (tsid >= 128) {
            wt_task(p, L, rs, v - 128, w, lane, request);
            TSA(7);
            if (w == 0) answer();
            DRAIN();
            __syncthreads();
            if (w == 0) raise();
        } else {
            unsigned ackv = 0u;
            bool raised = false;
            row_task<0>(p, L, rs, v, 1, [](int) { return 0; }, w, lane, tsid, request, [&](auto kc, int) {
                if constexpr (kc.value == 1) ack_ask(ackv);
                if constexpr (kc.value == 2) asm volatile("s_waitcnt vmcnt(0)" : "+v"(ackv) : : "memory");
                if constexpr (kc.value == 3) {
                    if (w == 0 && __builtin_amdgcn_readfirstlane(ackv) == token()) { raise(); raised = true; TSA(2); }
                }
            });
            TSA(7);
            if (w == 0 && !raised) { answer(); raise(); }
        }
        if (jj < 5) { TSA(8); }
        TS(23);
    }

    if constexpr (PB == 0) {
        if (FUSED && isC) {
            CIn in;
            v4u rsd[2];
            const int lc = 64 * w + lane;
            const unsigned vx = (lc >> 1) * XH0_TS + (lc & 1) * 16 + OFF_XH0 + 4 * q * XH0_RS, vw = lc * 16 + OFF_WT + 2 * q * 8192;
            CRaw kr;
            if (w == 0) pd_ask(vpd);
            c_tab_ld(kr, w, lane);
            if (w == 0) {
                asm volatile("s_waitcnt vmcnt(16)" : "+v"(pd));
                pd_wait(0, 32, vpd);
            }
            __syncthreads();
            TS(24);
            X_DELAY;
            const unsigned vrs = OFF_RS0 + lc * 32, vpw = OFF_PD + (bid & 7) * (64 * PD_ST) + (32 + (lane & 7)) * PD_ST;
            v4u pw;
            auto max4 = [](v4u v) { return max(max(v.x, v.y), max(v.z, v.w)); };
            unsigned spin = 0;
            const unsigned vl = (lc >> 3) * 4 * XH0_TS + (lc & 7) * 16 + OFF_XH0 + 4 * q * XH0_RS;
            in.xr[0] = in.xi[0] = in.xr[1] = in.xi[1] = rsd[0] = rsd[1] = pw = v4u{0u, 0u, 0u, 0u};
#pragma nounroll
            for (unsigned long long need = ~0ull; need != 0ull && spin < SPIN_CAP; ++spin) {
                unsigned long long sv;
                asm volatile("s_nop 1\n\ts_mov_b64 %7, exec\n\ts_mov_b64 exec, %8\n\t"
                             "global_load_dwordx4 %0, %13, %12 sc1\n\tglobal_load_dwordx4 %1, %14, %12 sc1\n\t"
                             "global_load_dwordx4 %2, %15, %12 sc1\n\tglobal_load_dwordx4 %3, %16, %12 sc1\n\t"
                             "global_load_dwordx4 %4, %10, %12 sc1\n\tglobal_load_dwordx4 %5, %10, %12 offset:16 sc1\n\t"
                             "s_mov_b64 exec, %7\n\tglobal_load_dwordx4 %6, %11, %12 sc1\n\ts_waitcnt vmcnt(0)"
                             : "+v"(in.xr[0]), "+v"(in.xi[0]), "+v"(in.xr[1]), "+v"(in.xi[1]), "+v"(rsd[0]), "+v"(rsd[1]), "+v"(pw), "=&s"(sv)
                             : "s"(need), "v"(vx), "v"(vrs), "v"(vpw), "s"(wsg), "v"(vl), "v"(vl + XH0_TS), "v"(vl + 2 * XH0_TS), "v"(vl + 3 * XH0_TS) : "memory");
                need = __builtin_amdgcn_ballot_w64(max(max(max(max4(in.xr[0]), max4(in.xi[0])), max(max4(in.xr[1]), max4(in.xi[1]))),
                                                       max(max4(rsd[0]), max4(rsd[1]))) == EMPTY);
                for (; __builtin_popcountll(need) > XGATE && spin < SPIN_CAP; ++spin) {
                    XSLEEP;
                    v4u r0, r1;
                    asm volatile("global_load_dwordx4 %0, %2, %3 sc1\n\tglobal_load_dwordx4 %1, %2, %3 offset:16 sc1\n\ts_waitcnt vmcnt(0)"
                                 : "=&v"(r0), "=&v"(r1) : "v"(vrs), "s"(wsg) : "memory");
                    if (__builtin_popcountll(__builtin_amdgcn_ballot_w64(max(max4(r0), max4(r1)) == EMPTY)) <= XGATE) break;
                }
            }
            {
                auto xch = [&](v4u &a, v4u &b, bool s, auto up, auto down) {
#pragma unroll
                    for (int d = 0; d < 4; ++d) {
                        const unsigned pa = __builtin_amdgcn_mov_dpp(a[d], up.value, 0xf, 0xf, true), pb = __builtin_amdgcn_mov_dpp(b[d], down.value, 0xf, 0xf, true);
                        a[d] = s ? pb : a[d];
                        b[d] = s ? b[d] : pa;
                    }
                };
                const int lf = fresh(lane);
                using q2 = std::integral_constant<int, 0x4E>;
                xch(in.xr[0], in.xi[0], lf & 2, q2{}, q2{});
                xch(in.xr[1], in.xi[1], lf & 2, q2{}, q2{});
                xch(in.xr[0], in.xr[1], lf & 4, std::integral_constant<int, 0x104>{}, std::integral_constant<int, 0x114>{});
                xch(in.xi[0], in.xi[1], lf & 4, std::integral_constant<int, 0x104>{}, std::integral_constant<int, 0x114>{});
            }
            TS(27);
            for (; spin < SPIN_CAP && any_lane(max4(pw) == EMPTY); ++spin)
                asm volatile("global_load_dwordx4 %0, %1, %2 sc1\n\ts_waitcnt vmcnt(0)" : "=v"(pw) : "v"(vpw), "s"(wsg) : "memory");
            ld_asm1(in.wr[0], wsg, vw);
            ld_asm1(in.wi[0], wsg, vw + 4096);
            ld_asm1(in.wr[1], wsg, vw + 8192);
            ld_asm1(in.wi[1], wsg, vw + 12288);
            CConst K;
            c_tab_use(K, kr);
#pragma unroll
            for (int h = 0; h < 4; ++h)
                in.rstd[h] = f32x2{u2f(rsd[h >> 1][(h & 1) * 2]), u2f(rsd[h >> 1][(h & 1) * 2 + 1])};
            TS(1);
            if (w == 0) pd_ask(vpd);
            phase_c<0, CTS1, true>(K, in, L, rs, q, OFF_XH1, w, lane, run1, voffr, [&](auto step) {
                if constexpr (step.value == 1) {
                    if (w == 0) { asm volatile("" : "+v"(pd)); pd_wait(52, 62, vpd); }
                    TS(25);
                }
                if constexpr (step.value == 2) pd_ask(vpd);
            });
            TS(2);
            asm volatile("s_waitcnt vmcnt(4)" : "+v"(pd));
            pd_wait(40, 52, vpd);
            TS(28);
            if constexpr (PE > 1) ring_fill<0, RING0>(run1, voffr);
        } else if (FUSED) {
            if (w == 0) { pd_poll(vpd); pd_wait(40, 62, vpd); NC_SLEEP; }
            __syncthreads();
            TS(25);
            {
                const unsigned wv = (3 * (slot >> 3) + (slot & 7) - 5) * 4 + w, ln = fresh(lane);
                const GAS unsigned char *w1f = wsg + OFF_W1F;
                constexpr unsigned LR = RING0 * 8, PW = 20 * LR / 48;
#pragma unroll
                for (unsigned k2 = 0; 64 * k2 < PW; ++k2) {
                    const unsigned n_ = wv * PW + min(ln + 64 * k2, PW - 1);
                    const unsigned r_ = n_ / LR;
                    asm volatile("global_load_dword a240, %0, %1" : : "v"((((JPERM >> (r_ & 28)) & 7) * 4 + (r_ & 3)) * 61440 + (n_ % LR) * 128), "s"(w1f) : "a240");
                }
                ring_fill<0, RING0>(run1, voffr);
            }
        } else if (isC) {
            CIn in;
            v4u rsd[2];
            const int lc = 64 * w + lane;
            const unsigned vx = (lc >> 1) * XH0_TS + (lc & 1) * 16 + OFF_XH0 + 4 * q * XH0_RS, vw = lc * 16 + OFF_WT + 2 * q * 8192;
            ld_asm(in.xr[0], wsg, vx);
            ld_asm(in.xi[0], wsg, vx + XH0_RS);
            ld_asm(in.xr[1], wsg, vx + 2 * XH0_RS);
            ld_asm(in.xi[1], wsg, vx + 3 * XH0_RS);
            ld_asm(rsd[0], wsg, OFF_RS0 + lc * 32);
            ld_asm(rsd[1], wsg, OFF_RS0 + lc * 32 + 16);
            ld_asm(in.wr[0], wsg, vw);
            ld_asm(in.wi[0], wsg, vw + 4096);
            ld_asm(in.wr[1], wsg, vw + 8192);
            ld_asm(in.wi[1], wsg, vw + 12288);
            CConst K;
            c_consts_ring<(PE > 1)>(K, w, lane, run1, voffr);
            if constexpr (PE > 1)
                asm volatile("s_waitcnt vmcnt(30)" : "+v"(in.xr[0]), "+v"(in.xi[0]), "+v"(in.xr[1]), "+v"(in.xi[1]), "+v"(rsd[0]), "+v"(rsd[1]),
                             "+v"(in.wr[0]), "+v"(in.wi[0]), "+v"(in.wr[1]), "+v"(in.wi[1]));
            else
                asm volatile("s_waitcnt vmcnt(0)" : "+v"(in.xr[0]), "+v"(in.xi[0]), "+v"(in.xr[1]), "+v"(in.xi[1]), "+v"(rsd[0]), "+v"(rsd[1]),
                             "+v"(in.wr[0]), "+v"(in.wi[0]), "+v"(in.wr[1]), "+v"(in.wi[1]));
#pragma unroll
            for (int h = 0; h < 4; ++h)
                in.rstd[h] = f32x2{u2f(rsd[h >> 1][(h & 1) * 2]), u2f(rsd[h >> 1][(h & 1) * 2 + 1])};
            TS(1);
            phase_c<(PE > 1) ? 1 : 0, CTS1>(K, in, L, rs, q, OFF_XH1, w, lane, run1, voffr, [](auto) {});
            TS(2);
        } else {
            if constexpr (PE > 1) ring_fill<0, 60>(run1, voffr);
        }
        TS(3);
    }
    if constexpr (PB <= 1 && PE > 1) {
        if constexpr (PB == 1) ring_fill<0, 60>(run1, voffr);
        v4u ra[25];
        constexpr int LATE_RING = FUSED ? RING0 : 60;
        phase_g1<false, LATE_RING>(p, L, rs, rsv, i, j, w, lane, run1, run2, LIGHT1, ra, [&](auto after) {
            if constexpr (FUSED) {
                if (w == 0) {
                    const unsigned vpd2 = OFF_PD2 + (bid & 7) * (32 * P2_ST) + min(lane, 11) * P2_ST;
                    if constexpr (!after.value) pd_ask(vpd2);
                    else { asm volatile("" : "+v"(pd)); pd_wait(0, 64, vpd2); }
                }
            }
        });
        TS(6);
        f32x4 acc[5][4];
        G2_DELAY;
        phase_g2<false>(p, L, rs, rsv, i, j, w, lane, acc);
        TS(11);
    }
    if constexpr (PB <= 2 && PE > 2) {
        if (isC) {
            CIn in;
            const int lc = 64 * w + fresh(lane);
            const int vw = lc * 16;
#pragma unroll
            for (int c = 0; c < 2; ++c) {
                in.wr[c] = ld_sc1(rs, vw, OFF_WT + (2 * q + c) * 8192);
                in.wi[c] = ld_sc1(rs, vw, OFF_WT + (2 * q + c) * 8192 + 4096);
            }
            const int vx = xh_lane_off(lc);
            const unsigned xrow = OFF_XH2 + 4 * q * XH_RS;
            v2u fl;
            const unsigned vfl = OFF_FLG + FLG_R2;
            flags_ld(fl, wsg, vfl);
            CConst K;
            c_consts_ring<(PE > 3)>(K, w, lane, run1, voffr);
            flags_wait<(PE > 3) ? 30 : 0>(fl, wsg, vfl);
            v4u xin[4], ssv[8];
            {
                const unsigned ssp = OFF_SSP;
                asm volatile(ASM_CIN_ISSUE ASM_CIN_WAIT
                             : "=&{v[128:131]}"(xin[0]), "=&{v[132:135]}"(xin[1]), "=&{v[136:139]}"(xin[2]), "=&{v[140:143]}"(xin[3]),
                               "=&{v[144:147]}"(ssv[0]), "=&{v[148:151]}"(ssv[1]), "=&{v[152:155]}"(ssv[2]), "=&{v[156:159]}"(ssv[3]),
                               "=&{v[160:163]}"(ssv[4]), "=&{v[164:167]}"(ssv[5]), "=&{v[168:171]}"(ssv[6]), "=&{v[172:175]}"(ssv[7])
                             : [vx] "v"(vx), [vw] "v"(vw), [rs] "s"(rsv), [xrow] "s"(xrow), [ssp] "s"(ssp) : ASM_CIN_CLOB);
            }
            asm volatile("" : "+v"(in.wr[0]), "+v"(in.wi[0]), "+v"(in.wr[1]), "+v"(in.wi[1]));
            in.xr[0] = xin[0]; in.xi[0] = xin[1]; in.xr[1] = xin[2]; in.xi[1] = xin[3];
#pragma unroll
            for (int h = 0; h < 4; ++h) {
                f32x2 s = bf_pair(ssv[0][h]);
#pragma unroll
                for (int jj = 1; jj < 8; ++jj) s += bf_pair(ssv[jj][h]);
                s = s * (1.0f / 640.0f) + 1e-5f;
                in.rstd[h] = f32x2{__builtin_amdgcn_rsqf(s[0]), __builtin_amdgcn_rsqf(s[1])};
            }
            TS(12);
            phase_c<(PE > 3) ? 1 : 0, CTS2>(K, in, L, rs, q, OFF_XH3, w, lane, run1, voffr, [](auto) {});
            TS(13);
            T2_DELAY;
        } else if constexpr (PE > 3) {
            {
                const int v = 8 * (3 * (slot >> 3) + (slot & 7) - 5) + (bid & 7);
                v4u t[10];
                ring_fill<0, LATE1>(run1, voffr);
                if (v < 48) w2t_load(p, t, v, w, fresh(lane));
                else if (v < 68) rb2_task(rs, v - 48, w, fresh(lane), []() {});
                __builtin_amdgcn_sched_barrier(0);
                ring_fill<LATE1, LATE2>(run1, voffr);
                if (v < 48) {
                    __syncthreads();
                    TS(7);
                    w2t_rest(L, rs, t, v, w, fresh(lane));
                }
                TS(8);
                ring_fill<LATE2, 60>(run1, voffr);
                TS(19);
                if (v < 68) {
                    DRAIN();
                    __syncthreads();
                    if (w == 0 && lane < 8) __builtin_amdgcn_raw_buffer_store_b32(1u, rs, lane * (32 * P2_ST) + flag_off(48 + v, P2_ST), OFF_PD2, AUX_SC1);
                }
                TS(29);
            }
            const int ln = fresh(lane);
            for (unsigned spin = 0; spin < SPIN_CAP; ++spin) {
                MEM_FENCE();
                const v4u v = ld_sc1(rs, ((ln >> 3) * 256 + 8 * i + (ln & 7)) * 16, OFF_SSP);
                if (!any_lane(max(max(v.x, v.y), max(v.z, v.w)) == EMPTY)) break;
                __builtin_amdgcn_s_sleep(4);
            }
        }
        TS(14);
    }
    if constexpr (PE > 3) {
        if constexpr (PB == 3) ring_fill<0, 60>(run1, voffr);
        v4u ra[25];
        phase_g1<true, 60>(p, L, rs, rsv, i, j, w, lane, run1, run3, LIGHT2, ra, [&](auto after) {
            if constexpr (FUSED) {
                if (w == 0) {
                    const unsigned vpd3 = OFF_PD2 + (bid & 7) * (32 * P2_ST) + min(12 + fresh(lane), 28) * P2_ST;
                    if constexpr (!after.value) pd_ask(vpd3);
                    else { asm volatile("" : "+v"(pd)); pd_wait(0, 64, vpd3); }
                }
            }
        });
        TS(17);
        f32x4 acc[5][4];
        phase_r(L, w, lane, run3, ra, acc);
        TS(18);
        phase_g2<true>(p, L, rs, rsv, i, j, w, lane, acc);
        TS(21);
    }
    if constexpr (FUSED) asm volatile(".Lk_end_%c0:" ::"i"(PB * 100 + PE * 10 + FUSED));
    TS_DUMP();
}

static void fill_params(Params &p, void *const *d_in, void *d_out, void *d_ws) {
    p.x = (const uint16_t *)d_in[0];
    p.wr = (const uint16_t *)d_in[1];
    p.wi = (const uint16_t *)d_in[2];
    p.w1 = (const uint16_t *)d_in[4];
    p.b1 = (const uint16_t *)d_in[5];
    p.w2 = (const uint16_t *)d_in[6];
    p.b2 = (const uint16_t *)d_in[7];
    p.out = (uint16_t *)d_out;
    p.ws = (unsigned char *)d_ws;
}
template <int PB, int PE, bool FUSED = false> static void launch_main(const Params &p, hipStream_t stream) {
    hipFuncSetAttribute((const void *)k_main<PB, PE, FUSED>, hipFuncAttributeMaxDynamicSharedMemorySize, L_BYTES);
    k_main<PB, PE, FUSED><<<256, 256, L_BYTES, stream>>>(p);
}

extern "C" void kernel_launch(void *const *d_in, const int *in_sizes, int n_in, void *d_out, int out_size, void *d_ws, size_t ws_size,
                              hipStream_t stream) {
    Params p;
    fill_params(p, d_in, d_out, d_ws);
    launch_main<0, 4, true>(p, stream);
}
```
